```python
import torch

def _softmax(x, axis=-1):
    return torch.softmax(x, dim=axis)
def _gelu(x):
    return torch.nn.functional.gelu(x, approximate="tanh")
def _silu(x):
    return torch.nn.functional.silu(x)
def _sigmoid(x):
    return torch.sigmoid(x)
def _layernorm(x, axis=-1, eps=1e-5):
    m = x.mean(dim=axis, keepdim=True)
    v = x.var(dim=axis, keepdim=True, unbiased=False)
    return (x - m) / torch.sqrt(v + eps)
def _scatter_add(tgt, idx, src, axis=0):
    return tgt.clone().index_add_(axis, idx, src)
def _segment_sum(src, seg, n):
    out = torch.zeros((n,) + src.shape[1:], dtype=src.dtype)
    return out.index_add_(0, seg, src)
def _pad(x, pw):
    flat = tuple(v for pair in reversed(pw) for v in pair)
    return torch.nn.functional.pad(x, flat)

kernel_name = 'sequential_preLN__compressed_kv_cla__dense_activation_gelu__prompt'

def setup_inputs():

    _w = (lambda a: (lambda t: t.to(torch.bfloat16) if t.is_floating_point() else t)(torch.as_tensor(a)))
    def _A_setup():
        B, H, Sq, Sk, D, M = 1, 32, 2048, 2048, 128, 512
        Dc = 4 * D
        return {
            "x": _w((lambda *s: torch.randn(*s, dtype=torch.bfloat16) * 0.05)(B, Sq, M)),
            "c_kv": _w((lambda *s: torch.randn(*s, dtype=torch.bfloat16) * 0.2)(B, Sk, Dc)),
            "w_q": _w((lambda *s: torch.randn(*s, dtype=torch.bfloat16) * 6.4)(M, H, D) * (M ** -0.5)),
            "w_uk": _w((lambda *s: torch.randn(*s, dtype=torch.bfloat16) * 12.8)(Dc, H, D) * (Dc ** -0.5)),
            "w_uv": _w((lambda *s: torch.randn(*s, dtype=torch.bfloat16) * 0.2)(Dc, H, D) * (Dc ** -0.5)),
            "w_o": _w((lambda *s: torch.randn(*s, dtype=torch.bfloat16) * 0.2)(H, D, M) * ((H * D) ** -0.5)),
        }
    _A_in = _A_setup()

    def _B_setup():
        B, S, D, F = 1, 2048, 512, 1376
        return {
            "x": _w((lambda *s: torch.randn(*s, dtype=torch.bfloat16) * 0.05)(B, S, D)),
            "w1": _w((lambda *s: torch.randn(*s, dtype=torch.bfloat16) * 0.4)(D, F) * (D ** -0.5)),
            "b1": _w((lambda *s: torch.randn(*s, dtype=torch.bfloat16) * 12.8)(F) * 0.01),
            "w2": _w((lambda *s: torch.randn(*s, dtype=torch.bfloat16) * 0.2)(F, D) * (F ** -0.5)),
            "b2": _w((lambda *s: torch.randn(*s, dtype=torch.bfloat16) * 1.6)(D) * 0.01),
        }
    _B_in = _B_setup()

    return {
        "A_x": _A_in["x"],
        "A_c_kv": _A_in["c_kv"],
        "A_w_q": _A_in["w_q"],
        "A_w_uk": _A_in["w_uk"],
        "A_w_uv": _A_in["w_uv"],
        "A_w_o": _A_in["w_o"],
        "B_x": _B_in["x"],
        "B_w1": _B_in["w1"],
        "B_b1": _B_in["b1"],
        "B_w2": _B_in["w2"],
        "B_b2": _B_in["b2"],
    }

def reference(A_x, A_c_kv, A_w_q, A_w_uk, A_w_uv, A_w_o, B_x, B_w1, B_b1, B_w2, B_b2):
    def _A_ref(x, c_kv, w_q, w_uk, w_uv, w_o):
        B, Sq, M = x.shape[0], x.shape[1], x.shape[2]
        Sk = c_kv.shape[1]
        H, D = w_q.shape[1], w_q.shape[2]
        q = torch.einsum("bqm,mhd->bhqd", x, w_q)
        k = torch.einsum("bkc,chd->bhkd", c_kv, w_uk)
        v = torch.einsum("bkc,chd->bhkd", c_kv, w_uv)
        scores = torch.einsum("bhqd,bhkd->bhqk", q, k) * (D ** -0.5)
        off = Sk - Sq
        qi = torch.arange(Sq)[:, None]
        kj = torch.arange(Sk)[None, :]
        cmask = kj <= (qi + off)
        scores = torch.where(cmask[None, None], scores, -1e9)
        p = _softmax(scores, axis=-1)
        ctx = torch.einsum("bhqk,bhkd->bhqd", p, v)
        return torch.einsum("bhqd,hdm->bqm", ctx, w_o)

    def _B_ref(x, w1, b1, w2, b2):
        h = torch.einsum("bsd,df->bsf", x, w1) + b1
        h = _gelu(h)
        return torch.einsum("bsf,fd->bsd", h, w2) + b2

    x = A_x
    h = x + _A_ref(_layernorm(x, axis=-1), A_c_kv, A_w_q, A_w_uk, A_w_uv, A_w_o)
    h2 = h + _B_ref(_layernorm(h, axis=-1), B_w1, B_b1, B_w2, B_b2)
    return h2
```

```cpp
#include <hip/hip_runtime.h>
#include <hip/hip_bf16.h>
#include <stdint.h>

typedef short bf16x8 __attribute__((ext_vector_type(8)));
typedef short s16x4 __attribute__((ext_vector_type(4)));
typedef float f32x4 __attribute__((ext_vector_type(4)));
typedef float f32x16 __attribute__((ext_vector_type(16)));
typedef unsigned u32x4 __attribute__((ext_vector_type(4)));
typedef unsigned u32x2 __attribute__((ext_vector_type(2)));
typedef unsigned short u16;

#define SBAR() __builtin_amdgcn_sched_barrier(0)

constexpr int SEQ = 2048, DM = 512, NH = 32, HD = 128, DC = 512, DFF = 1376, DFFP = 1408;

__device__ __forceinline__ unsigned cvtpk(float lo, float hi) {
    typedef float f32x2_ __attribute__((ext_vector_type(2))); typedef __bf16 bf16x2_ __attribute__((ext_vector_type(2)));
    f32x2_ v = {lo, hi}; bf16x2_ b = __builtin_convertvector(v, bf16x2_);
    return __builtin_bit_cast(unsigned, b);
}
__device__ __forceinline__ float bf2f(u16 v) { return __uint_as_float(((unsigned)v) << 16); }
__device__ __forceinline__ float bfr(float v) { return __uint_as_float(cvtpk(v, v) << 16); }

__device__ __forceinline__ float wave_sum(float v) {
#pragma unroll
    for (int o = 32; o > 0; o >>= 1) v += __shfl_xor(v, o);
    return v;
}
__device__ __forceinline__ void ln8(float* x) {
    float s = 0;
#pragma unroll
    for (int i = 0; i < 8; ++i) s += x[i];
    s = wave_sum(s);
    const float mean = s * (1.f / 512.f);
    float q = 0;
#pragma unroll
    for (int i = 0; i < 8; ++i) { const float d = x[i] - mean; q += d * d; }
    q = wave_sum(q);
    const float mb = bfr(mean);
    const float vb = bfr(q * (1.f / 512.f));
    const float den = bfr(sqrtf(bfr(vb + 1e-5f)));
#pragma unroll
    for (int i = 0; i < 8; ++i) x[i] = bfr(bfr(x[i] - mb) / den);
}
__device__ __forceinline__ void unpack8(u32x4 w, float* x) {
#pragma unroll
    for (int i = 0; i < 4; ++i) { x[2 * i] = __uint_as_float(w[i] << 16); x[2 * i + 1] = __uint_as_float(w[i] & 0xffff0000u); }
}
__device__ __forceinline__ u32x4 pack8f(const float* x) {
    u32x4 w = {cvtpk(x[0], x[1]), cvtpk(x[2], x[3]), cvtpk(x[4], x[5]), cvtpk(x[6], x[7])};
    return w;
}

__global__ __launch_bounds__(256) void ln1_kernel(const u16* __restrict__ x, u16* __restrict__ xn) {
    const int row = blockIdx.x * 4 + (threadIdx.x >> 6), lane = threadIdx.x & 63;
    u32x4 w = *(const u32x4*)(x + (size_t)row * DM + lane * 8);
    float v[8]; unpack8(w, v); ln8(v);
    *(u32x4*)(xn + (size_t)row * DM + lane * 8) = pack8f(v);
}

template <int KS>
__global__ __launch_bounds__(256) void oreduce_ln2_kernel(const float* __restrict__ part, const u16* __restrict__ x,
                                                          u16* __restrict__ h, u16* __restrict__ hn) {
    const int row = blockIdx.x * 4 + (threadIdx.x >> 6), lane = threadIdx.x & 63;
    float a[8];
#pragma unroll
    for (int i = 0; i < 8; ++i) a[i] = 0.f;
#pragma unroll
    for (int s = 0; s < KS; ++s) {
        const float* p = part + ((size_t)s * SEQ + row) * DM + lane * 8;
        f32x4 p0 = *(const f32x4*)p, p1 = *(const f32x4*)(p + 4);
#pragma unroll
        for (int i = 0; i < 4; ++i) { a[i] += p0[i]; a[4 + i] += p1[i]; }
    }
    u32x4 w = *(const u32x4*)(x + (size_t)row * DM + lane * 8);
    float v[8]; unpack8(w, v);
#pragma unroll
    for (int i = 0; i < 8; ++i) v[i] = bfr(v[i] + bfr(a[i]));
    *(u32x4*)(h + (size_t)row * DM + lane * 8) = pack8f(v);
    ln8(v);
    *(u32x4*)(hn + (size_t)row * DM + lane * 8) = pack8f(v);
}

enum { EPI_QKV = 0, EPI_PART = 1, EPI_GELU = 2, EPI_FFN2 = 3 };
struct GemmArgs {
    const u16* A[3]; const u16* B[3]; void* C[3];
    const u16* bias; const u16* res;
    int lda, ldb, M, N, K, Kv, nTn, ksplit;
};

__device__ __forceinline__ int a_lds_byte(int r, int c) {
    const int st = (r >> 4) * 2 + (c >> 5), ob = (r & 15) * 64 + (c & 31) * 2;
    return st * 1024 + (ob ^ (((ob >> 9) & 1) << 5));
}
__device__ __forceinline__ void a_stage_rc(int b, int& R, int& C) {
    const int st = b >> 10, sb = b & 1023, swz = sb ^ (((sb >> 9) & 1) << 5);
    R = (st >> 1) * 16 + (swz >> 6); C = (st & 1) * 32 + ((swz & 63) >> 1);
}

template <int BM, int BN, int WM, int WN, int EPI>
__global__ __launch_bounds__(WM * WN * 64) void gemm_kernel(GemmArgs g) {
    constexpr int NT = WM * WN * 64, TM = BM / WM, TN = BN / WN, MI = TM / 16, NI = TN / 16;
    constexpr int A_BYTES = BM * 64 * 2, B_BYTES = BN * 64 * 2, NSUB = BN / 32;
    extern __shared__ __attribute__((aligned(16))) char smem[];
    const int tid = threadIdx.x, wid = tid >> 6, lane = tid & 63, wm = wid / WN, wn = wid % WN;
    const int tn = blockIdx.x % g.nTn, tm = blockIdx.x / g.nTn, z = blockIdx.y;
    const int mat = (EPI == EPI_QKV) ? z : 0, ksp = (EPI == EPI_PART) ? z : 0;
    const u16* A = g.A[mat]; const u16* B = g.B[mat];
    const int m0 = tm * BM, n0 = tn * BN, kbase = ksp * g.K, nkt = g.K / 64;

    auto stage = [&](int kt, int buf) {
        char* sa = smem + buf * (A_BYTES + B_BYTES); char* sb = sa + A_BYTES;
        const int k0 = kbase + kt * 64;
#pragma unroll
        for (int i = 0; i < A_BYTES / (NT * 16); ++i) {
            const int b = (i * NT + tid) * 16; int R, C; a_stage_rc(b, R, C);
            __builtin_amdgcn_global_load_lds((const unsigned*)(A + (size_t)(m0 + R) * g.lda + k0 + C), (unsigned*)(sa + b), 16, 0, 0);
        }
#pragma unroll
        for (int i = 0; i < B_BYTES / (NT * 16); ++i) {
            const int P = i * NT + tid, s = P >> 5, r7 = (P >> 2) & 7, cx = P & 3;
            const int row = 8 * (s / NSUB) + r7, ch = 4 * (s % NSUB) + (cx ^ ((row >> 2) & 3));
            int kr = k0 + row; if (kr > g.Kv - 1) kr = g.Kv - 1;
            int nc = n0 + ch * 8; if (nc > g.N - 8) nc = g.N - 8;
            __builtin_amdgcn_global_load_lds((const unsigned*)(B + (size_t)kr * g.ldb + nc), (unsigned*)(sb + P * 16), 16, 0, 0);
        }
    };

    f32x4 acc[MI][NI];
#pragma unroll
    for (int i = 0; i < MI; ++i)
#pragma unroll
        for (int j = 0; j < NI; ++j) acc[i][j] = f32x4{0.f, 0.f, 0.f, 0.f};

    const int g4 = lane >> 4, i16 = lane & 15, q4 = i16 >> 2, p4 = i16 & 3;
    const int a_base = a_lds_byte(i16, 8 * g4);
    int b_base[2][2];
#pragma unroll
    for (int t = 0; t < 2; ++t)
#pragma unroll
        for (int par = 0; par < 2; ++par) {
            const int row = 8 * g4 + 4 * t + q4, chl = 2 * par + (p4 >> 1);
            b_base[t][par] = 512 * (g4 * NSUB + (wn * TN >> 5)) + 64 * (4 * t + q4) + 16 * (chl ^ ((row >> 2) & 3)) + 8 * (p4 & 1);
        }

    stage(0, 0);
    for (int kt = 0; kt < nkt; ++kt) {
        asm volatile("s_waitcnt vmcnt(0)" ::: "memory");
        __syncthreads();
        if (kt + 1 < nkt) stage(kt + 1, (kt + 1) & 1);
        const char* sa = smem + (kt & 1) * (A_BYTES + B_BYTES);
        const unsigned sbo = (unsigned)(uintptr_t)(sa + A_BYTES);
#pragma unroll
        for (int ks = 0; ks < 2; ++ks) {
            bf16x8 af[MI], bfr_[NI];
#pragma unroll
            for (int mi = 0; mi < MI; ++mi) af[mi] = *(const bf16x8*)(sa + a_base + ((wm * MI + mi) * 2 + ks) * 1024);
#pragma unroll
            for (int ni = 0; ni < NI; ++ni) {
                const int off = 512 * (4 * ks * NSUB + (ni >> 1));
                s16x4 lo, hi;
                const unsigned a0 = sbo + b_base[0][ni & 1], a1 = sbo + b_base[1][ni & 1];
                asm volatile("ds_read_b64_tr_b16 %0, %1 offset:%2" : "=&v"(lo) : "v"(a0), "i"(off) : "memory");
                asm volatile("ds_read_b64_tr_b16 %0, %1 offset:%2" : "=&v"(hi) : "v"(a1), "i"(off) : "memory");
                bfr_[ni] = (bf16x8){lo[0], lo[1], lo[2], lo[3], hi[0], hi[1], hi[2], hi[3]};
            }
            asm volatile("s_waitcnt lgkmcnt(0)" ::: "memory"); SBAR();
#pragma unroll
            for (int mi = 0; mi < MI; ++mi)
#pragma unroll
                for (int ni = 0; ni < NI; ++ni)
                    acc[mi][ni] = __builtin_amdgcn_mfma_f32_16x16x32_bf16(bfr_[ni], af[mi], acc[mi][ni], 0, 0, 0);
        }
    }
#pragma unroll
    for (int mi = 0; mi < MI; ++mi) {
        const int m = m0 + wm * TM + 16 * mi + i16;
#pragma unroll
        for (int ni = 0; ni < NI; ++ni) {
            const int n = n0 + wn * TN + 16 * ni + 4 * g4;
            f32x4 v = acc[mi][ni];
            if constexpr (EPI == EPI_QKV) {
                u32x2 w = {cvtpk(v[0], v[1]), cvtpk(v[2], v[3])};
                *(u32x2*)((u16*)g.C[mat] + ((size_t)(n >> 7) * SEQ + m) * HD + (n & 127)) = w;
            } else if constexpr (EPI == EPI_PART) {
                *(f32x4*)((float*)g.C[0] + ((size_t)ksp * g.M + m) * g.N + n) = v;
            } else if constexpr (EPI == EPI_GELU) {
                float o[4];
                if (n < g.N) {
                    u32x2 bw = *(const u32x2*)(g.bias + n);
                    const float b[4] = {__uint_as_float(bw[0] << 16), __uint_as_float(bw[0] & 0xffff0000u),
                                        __uint_as_float(bw[1] << 16), __uint_as_float(bw[1] & 0xffff0000u)};
#pragma unroll
                    for (int j = 0; j < 4; ++j) {
                        const float xx = bfr(bfr(v[j]) + b[j]);
                        const float u = 0.7978845608028654f * (xx + 0.044715f * xx * xx * xx);
                        const float th = 1.f - 2.f / (__expf(2.f * u) + 1.f);
                        o[j] = 0.5f * xx * (1.f + th);
                    }
                } else { o[0] = o[1] = o[2] = o[3] = 0.f; }
                if (n < DFFP) {
                    u32x2 w = {cvtpk(o[0], o[1]), cvtpk(o[2], o[3])};
                    *(u32x2*)((u16*)g.C[0] + (size_t)m * DFFP + n) = w;
                }
            } else {
                u32x2 bw = *(const u32x2*)(g.bias + n);
                u32x2 rw = *(const u32x2*)(g.res + (size_t)m * DM + n);
                const float b[4] = {__uint_as_float(bw[0] << 16), __uint_as_float(bw[0] & 0xffff0000u),
                                    __uint_as_float(bw[1] << 16), __uint_as_float(bw[1] & 0xffff0000u)};
                const float r[4] = {__uint_as_float(rw[0] << 16), __uint_as_float(rw[0] & 0xffff0000u),
                                    __uint_as_float(rw[1] << 16), __uint_as_float(rw[1] & 0xffff0000u)};
                float o[4];
#pragma unroll
                for (int j = 0; j < 4; ++j) o[j] = r[j] + bfr(bfr(v[j]) + b[j]);
                u32x2 w = {cvtpk(o[0], o[1]), cvtpk(o[2], o[3])};
                *(u32x2*)((u16*)g.C[0] + (size_t)m * DM + n) = w;
            }
        }
    }
}

constexpr float SCALE = 0.08838834764831845f;
constexpr int ATT_LDS = 4 * 32768 + 8 * 64 * 4;
__device__ __forceinline__ int crow(int r, int hi) { return (r & 3) + 8 * (r >> 2) + 4 * hi; }
__device__ __forceinline__ int v_rd_base(int lane) { return ((lane & 3) << 3) | (((lane >> 2) & 3) << 6) | (((lane >> 4) & 1) << 5) | (((lane >> 5) & 1) << 8); }

__global__ __launch_bounds__(512, 1) void attn_kernel(const u16* __restrict__ Q, const u16* __restrict__ K, const u16* __restrict__ V,
                                                      u16* __restrict__ ctx) {
    extern __shared__ __attribute__((aligned(16))) char lds[];
    const int tid = threadIdx.x, wid = __builtin_amdgcn_readfirstlane(tid >> 6), lane = tid & 63, r32 = lane & 31, hi = lane >> 5;
    const int rg = wid & 3, par = wid >> 2;
    const int h = blockIdx.x >> 3, xq = blockIdx.x & 7;
    const u16* Kh = K + (size_t)h * SEQ * HD; const u16* Vh = V + (size_t)h * SEQ * HD;
    char* K_lds = lds; char* V_lds = lds + 65536;
    float* al_l = (float*)(lds + 4 * 32768) + wid * 64;
    const int vb0 = (int)(uintptr_t)V_lds + par * 16384 + v_rd_base(lane);

    const unsigned koff = (unsigned)((tid >> 4) * 256 + (((tid & 15) ^ ((tid >> 4) & 7)) << 4));
    unsigned voff;
    { const int sub = tid >> 5, r7 = (tid >> 2) & 7, c8 = tid & 3, kk = (sub >> 2) * 8 + r7;
      const int k = (kk & ~0xC) | ((kk & 4) << 1) | ((kk & 8) >> 1);
      voff = (unsigned)(k * 256 + ((sub & 3) * 32 + c8 * 8) * 2); }
    auto stage = [&](int s, int buf) {
        const char* kb_ = (const char*)Kh + (size_t)s * 32768; const char* vb_ = (const char*)Vh + (size_t)s * 32768;
#pragma unroll
        for (int i = 0; i < 4; ++i)
            __builtin_amdgcn_global_load_lds((const unsigned*)(kb_ + koff + i * 8192), (unsigned*)(K_lds + buf * 32768 + i * 8192 + tid * 16), 16, 0, 0);
#pragma unroll
        for (int i = 0; i < 4; ++i)
            __builtin_amdgcn_global_load_lds((const unsigned*)(vb_ + voff + i * 8192), (unsigned*)(V_lds + buf * 32768 + i * 8192 + tid * 16), 16, 0, 0);
    };

    for (int pass = 0; pass < 2; ++pass) {
        const int qb = pass ? 15 - xq : xq, nsteps = qb + 1;
        const int qrow0 = qb * 128 + rg * 32;
        bf16x8 qr[8];
#pragma unroll
        for (int d0 = 0; d0 < 8; ++d0) qr[d0] = *(const bf16x8*)(Q + ((size_t)h * SEQ + qrow0 + r32) * HD + d0 * 16 + hi * 8);
        float m_reg = -1e30f, l_reg = 0.f; f32x16 o[4] = {};
        __syncthreads();
        stage(0, 0);
        for (int s = 0; s < nsteps; ++s) {
            asm volatile("s_waitcnt vmcnt(0)" ::: "memory");
            __syncthreads();
            if (s + 1 < nsteps) stage(s + 1, (s + 1) & 1);
            const int buf = s & 1, kbase = 128 * s + 64 * par;
            if (kbase > qrow0 + 31) continue;
            f32x16 p0 = {}, p1 = {};
            {
                const char* kb[4];
#pragma unroll
                for (int dd = 0; dd < 4; ++dd) kb[dd] = K_lds + buf * 32768 + (par * 64 + r32) * 256 + (((dd * 16 + hi * 8) * 2) ^ ((r32 & 7) << 4));
#pragma unroll
                for (int d0 = 0; d0 < 8; ++d0) {
                    const char* a = kb[d0 & 3] + (d0 >> 2) * 128;
                    bf16x8 b0 = *(const bf16x8*)a, b1 = *(const bf16x8*)(a + 32 * 256);
                    p0 = __builtin_amdgcn_mfma_f32_32x32x16_bf16(b0, qr[d0], p0, 0, 0, 0);
                    p1 = __builtin_amdgcn_mfma_f32_32x32x16_bf16(b1, qr[d0], p1, 0, 0, 0);
                }
            }
#pragma unroll
            for (int r = 0; r < 16; r += 2) {
                unsigned u = cvtpk(p0[r], p0[r + 1]);
                float a = __uint_as_float(u << 16) * SCALE, b = __uint_as_float(u & 0xffff0000u) * SCALE;
                u = cvtpk(a, b); p0[r] = __uint_as_float(u << 16); p0[r + 1] = __uint_as_float(u & 0xffff0000u);
                u = cvtpk(p1[r], p1[r + 1]);
                a = __uint_as_float(u << 16) * SCALE; b = __uint_as_float(u & 0xffff0000u) * SCALE;
                u = cvtpk(a, b); p1[r] = __uint_as_float(u << 16); p1[r + 1] = __uint_as_float(u & 0xffff0000u);
            }
            if (kbase + 63 > qrow0) {
                const int dq = qrow0 + r32 - kbase - 4 * hi; const float NEG = -__builtin_inff();
#pragma unroll
                for (int r = 0; r < 16; ++r) { const int c = (r & 3) + 8 * (r >> 2);
                    if (dq - c < 0) p0[r] = NEG;
                    if (dq - c - 32 < 0) p1[r] = NEG; }
            }
            float pmax = p0[0];
#pragma unroll
            for (int r = 1; r < 16; ++r) pmax = fmaxf(pmax, p0[r]);
#pragma unroll
            for (int r = 0; r < 16; ++r) pmax = fmaxf(pmax, p1[r]);
            { auto rr = __builtin_amdgcn_permlane32_swap(__float_as_uint(pmax), __float_as_uint(pmax), false, false);
              pmax = fmaxf(__uint_as_float(rr[0]), __uint_as_float(rr[1])); }
            constexpr float L2E = 1.4426950408889634f;
            const float mn = fmaxf(m_reg, pmax);
            const float alpha = __builtin_amdgcn_exp2f((m_reg - mn) * L2E);
            m_reg = mn;
            const float mnL = -mn * L2E;
            float ps = 0.f;
#pragma unroll
            for (int r = 0; r < 16; ++r) { p0[r] = __builtin_amdgcn_exp2f(fmaf(p0[r], L2E, mnL)); ps += p0[r]; }
#pragma unroll
            for (int r = 0; r < 16; ++r) { p1[r] = __builtin_amdgcn_exp2f(fmaf(p1[r], L2E, mnL)); ps += p1[r]; }
            { auto rr = __builtin_amdgcn_permlane32_swap(__float_as_uint(ps), __float_as_uint(ps), false, false);
              ps = __uint_as_float(rr[0]) + __uint_as_float(rr[1]); }
            l_reg = l_reg * alpha + ps;
            if (__any(alpha < 1.f)) {
                if (hi == 0) al_l[r32] = alpha;
                asm volatile("s_waitcnt lgkmcnt(0)" ::: "memory");
#pragma unroll
                for (int d = 0; d < 4; ++d)
#pragma unroll
                    for (int r = 0; r < 16; ++r) o[d][r] *= al_l[crow(r, hi)];
            }
            bf16x8 pa0, pa1, pa2, pa3;
#define PK4(P, B_, OUT) do { unsigned a0 = cvtpk(P[B_+0], P[B_+1]), a1 = cvtpk(P[B_+2], P[B_+3]);                          \
        unsigned b0 = cvtpk(P[B_+4], P[B_+5]), b1 = cvtpk(P[B_+6], P[B_+7]);                                             \
        auto r0 = __builtin_amdgcn_permlane32_swap(a0, b0, false, false); auto r1 = __builtin_amdgcn_permlane32_swap(a1, b1, false, false); \
        u32x4 w = {r0[0], r1[0], r0[1], r1[1]}; OUT = *reinterpret_cast<bf16x8*>(&w); } while (0)
            PK4(p0, 0, pa0); PK4(p0, 8, pa1); PK4(p1, 0, pa2); PK4(p1, 8, pa3);
#undef PK4
#define TRRD(dst, off) asm volatile("ds_read_b64_tr_b16 %0, %1 offset:%2" : "=&v"(dst) : "v"(vb), "i"(off) : "memory")
#define PV_D0(d0) do { s16x4 l0, l1, l2, l3, h0, h1, h2, h3; constexpr int b_ = (d0) * 512; \
        TRRD(l0, b_); TRRD(h0, b_ + 2048); TRRD(l1, b_ + 4096); TRRD(h1, b_ + 6144); TRRD(l2, b_ + 8192); TRRD(h2, b_ + 10240); TRRD(l3, b_ + 12288); TRRD(h3, b_ + 14336); \
        asm volatile("s_waitcnt lgkmcnt(0)" ::: "memory"); SBAR(); \
        o[d0] = __builtin_amdgcn_mfma_f32_32x32x16_bf16(pa0, (bf16x8){l0[0], l0[1], l0[2], l0[3], h0[0], h0[1], h0[2], h0[3]}, o[d0], 0, 0, 0);   \
        o[d0] = __builtin_amdgcn_mfma_f32_32x32x16_bf16(pa1, (bf16x8){l1[0], l1[1], l1[2], l1[3], h1[0], h1[1], h1[2], h1[3]}, o[d0], 0, 0, 0);   \
        o[d0] = __builtin_amdgcn_mfma_f32_32x32x16_bf16(pa2, (bf16x8){l2[0], l2[1], l2[2], l2[3], h2[0], h2[1], h2[2], h2[3]}, o[d0], 0, 0, 0);   \
        o[d0] = __builtin_amdgcn_mfma_f32_32x32x16_bf16(pa3, (bf16x8){l3[0], l3[1], l3[2], l3[3], h3[0], h3[1], h3[2], h3[3]}, o[d0], 0, 0, 0); } while (0)
            { const int vb = vb0 + buf * 32768; PV_D0(0); PV_D0(1); PV_D0(2); PV_D0(3); }
#undef PV_D0
#undef TRRD
        }
        __syncthreads();
        float* cO = (float*)lds + rg * (32 * 128 + 64);
        if (par == 1) {
#pragma unroll
            for (int d = 0; d < 4; ++d)
#pragma unroll
                for (int r = 0; r < 16; ++r) cO[crow(r, hi) * 128 + d * 32 + r32] = o[d][r];
            if (hi == 0) { cO[4096 + r32] = m_reg; cO[4096 + 32 + r32] = l_reg; }
        }
        __syncthreads();
        if (par == 0) {
            constexpr float L2E = 1.4426950408889634f;
            const float m1 = cO[4096 + r32], l1 = cO[4096 + 32 + r32];
            const float mm = fmaxf(m_reg, m1);
            const float a0 = __builtin_amdgcn_exp2f((m_reg - mm) * L2E), a1 = __builtin_amdgcn_exp2f((m1 - mm) * L2E);
            const float linv = 1.f / (l_reg * a0 + l1 * a1);
            if (hi == 0) { al_l[r32] = a0 * linv; al_l[32 + r32] = a1 * linv; }
            asm volatile("s_waitcnt lgkmcnt(0)" ::: "memory");
#pragma unroll
            for (int r = 0; r < 16; ++r) {
                const int orow = crow(r, hi); const float c0 = al_l[orow], c1 = al_l[32 + orow];
#pragma unroll
                for (int d = 0; d < 4; ++d) {
                    const float v = o[d][r] * c0 + cO[orow * 128 + d * 32 + r32] * c1;
                    const float vn = __shfl_xor(v, 1);
                    if ((r32 & 1) == 0) *(unsigned*)(ctx + (size_t)(qrow0 + orow) * (NH * HD) + h * HD + d * 32 + r32) = cvtpk(v, vn);
                }
            }
        }
    }
}

extern "C" void kernel_launch(void* const* d_in, const int* in_sizes, int n_in, void* d_out, int out_size, void* d_ws, size_t ws_size,
                              hipStream_t stream) {
    const u16* x = (const u16*)d_in[0]; const u16* ckv = (const u16*)d_in[1];
    const u16* wq = (const u16*)d_in[2]; const u16* wuk = (const u16*)d_in[3]; const u16* wuv = (const u16*)d_in[4]; const u16* wo = (const u16*)d_in[5];
    const u16* w1 = (const u16*)d_in[7]; const u16* b1 = (const u16*)d_in[8]; const u16* w2 = (const u16*)d_in[9]; const u16* b2 = (const u16*)d_in[10];
    u16* out = (u16*)d_out;
    char* ws = (char*)d_ws;
    u16* xn = (u16*)ws;                                   ws += (size_t)SEQ * DM * 2;
    u16* qb = (u16*)ws;                                   ws += (size_t)SEQ * NH * HD * 2;
    u16* kb = (u16*)ws;                                   ws += (size_t)SEQ * NH * HD * 2;
    u16* vb = (u16*)ws;                                   ws += (size_t)SEQ * NH * HD * 2;
    u16* ctx = (u16*)ws;                                  ws += (size_t)SEQ * NH * HD * 2;
    u16* hbuf = (u16*)ws;                                 ws += (size_t)SEQ * DM * 2;
    u16* hn = (u16*)ws;                                   ws += (size_t)SEQ * DM * 2;
    u16* fbuf = (u16*)ws;                                 ws += (size_t)SEQ * DFFP * 2;
    float* part = (float*)ws;                             ws += (size_t)4 * SEQ * DM * 4;

    constexpr int KS = 4;
    auto gq = gemm_kernel<128, 256, 2, 4, EPI_QKV>;
    auto go = gemm_kernel<128, 128, 2, 4, EPI_PART>;
    auto g1 = gemm_kernel<128, 128, 2, 4, EPI_GELU>;
    auto g2 = gemm_kernel<128, 64, 4, 2, EPI_FFN2>;
    (void)hipFuncSetAttribute((const void*)gq, hipFuncAttributeMaxDynamicSharedMemorySize, 2 * (128 + 256) * 128);
    (void)hipFuncSetAttribute((const void*)go, hipFuncAttributeMaxDynamicSharedMemorySize, 2 * (128 + 128) * 128);
    (void)hipFuncSetAttribute((const void*)g1, hipFuncAttributeMaxDynamicSharedMemorySize, 2 * (128 + 128) * 128);
    (void)hipFuncSetAttribute((const void*)g2, hipFuncAttributeMaxDynamicSharedMemorySize, 2 * (128 + 64) * 128);
    (void)hipFuncSetAttribute((const void*)attn_kernel, hipFuncAttributeMaxDynamicSharedMemorySize, ATT_LDS);

    ln1_kernel<<<SEQ / 4, 256, 0, stream>>>(x, xn);
    {
        GemmArgs g = {};
        g.A[0] = xn; g.A[1] = ckv; g.A[2] = ckv; g.B[0] = wq; g.B[1] = wuk; g.B[2] = wuv; g.C[0] = qb; g.C[1] = kb; g.C[2] = vb;
        g.lda = DM; g.ldb = NH * HD; g.M = SEQ; g.N = NH * HD; g.K = DM; g.Kv = DM; g.ksplit = 1;
        g.nTn = (NH * HD) / 256;
        gq<<<dim3(g.nTn * (SEQ / 128), 3), 512, 2 * (128 + 256) * 128, stream>>>(g);
    }
    attn_kernel<<<NH * 8, 512, ATT_LDS, stream>>>(qb, kb, vb, ctx);
    {
        GemmArgs g = {};
        g.A[0] = ctx; g.B[0] = wo; g.C[0] = part; g.lda = NH * HD; g.ldb = DM; g.M = SEQ; g.N = DM; g.K = NH * HD / KS; g.Kv = NH * HD;
        g.nTn = DM / 128; g.ksplit = KS;
        go<<<dim3(g.nTn * (SEQ / 128), KS), 512, 2 * (128 + 128) * 128, stream>>>(g);
    }
    oreduce_ln2_kernel<KS><<<SEQ / 4, 256, 0, stream>>>(part, x, hbuf, hn);
    {
        GemmArgs g = {};
        g.A[0] = hn; g.B[0] = w1; g.C[0] = fbuf; g.bias = b1; g.lda = DM; g.ldb = DFF; g.M = SEQ; g.N = DFF; g.K = DM; g.Kv = DM;
        g.nTn = (DFF + 127) / 128; g.ksplit = 1;
        g1<<<dim3(g.nTn * (SEQ / 128), 1), 512, 2 * (128 + 128) * 128, stream>>>(g);
    }
    {
        GemmArgs g = {};
        g.A[0] = fbuf; g.B[0] = w2; g.C[0] = out; g.bias = b2; g.res = hbuf; g.lda = DFFP; g.ldb = DM; g.M = SEQ; g.N = DM; g.K = DFFP; g.Kv = DFF;
        g.nTn = DM / 64; g.ksplit = 1;
        g2<<<dim3(g.nTn * (SEQ / 128), 1), 512, 2 * (128 + 64) * 128, stream>>>(g);
    }
}
```

```cpp
#include <hip/hip_runtime.h>
#include <hip/hip_bf16.h>
#include <stdint.h>

typedef short bf16x8 __attribute__((ext_vector_type(8)));
typedef short s16x4 __attribute__((ext_vector_type(4)));
typedef float f32x4 __attribute__((ext_vector_type(4)));
typedef float f32x16 __attribute__((ext_vector_type(16)));
typedef unsigned u32x4 __attribute__((ext_vector_type(4)));
typedef unsigned u32x2 __attribute__((ext_vector_type(2)));
typedef unsigned short u16;

#define SBAR() __builtin_amdgcn_sched_barrier(0)

constexpr int SEQ = 2048, DM = 512, NH = 32, HD = 128, DC = 512, DFF = 1376, DFFP = 1408;

__device__ __forceinline__ unsigned cvtpk(float lo, float hi) {
    typedef float f32x2_ __attribute__((ext_vector_type(2))); typedef __bf16 bf16x2_ __attribute__((ext_vector_type(2)));
    f32x2_ v = {lo, hi}; bf16x2_ b = __builtin_convertvector(v, bf16x2_);
    return __builtin_bit_cast(unsigned, b);
}
__device__ __forceinline__ float bf2f(u16 v) { return __uint_as_float(((unsigned)v) << 16); }
__device__ __forceinline__ float bfr(float v) { return __uint_as_float(cvtpk(v, v) << 16); }

__device__ __forceinline__ float wave_sum(float v) {
#pragma unroll
    for (int o = 32; o > 0; o >>= 1) v += __shfl_xor(v, o);
    return v;
}
__device__ __forceinline__ void ln8(float* x) {
    float s = 0;
#pragma unroll
    for (int i = 0; i < 8; ++i) s += x[i];
    s = wave_sum(s);
    const float mean = s * (1.f / 512.f);
    float q = 0;
#pragma unroll
    for (int i = 0; i < 8; ++i) { const float d = x[i] - mean; q += d * d; }
    q = wave_sum(q);
    const float mb = bfr(mean);
    const float vb = bfr(q * (1.f / 512.f));
    const float den = bfr(sqrtf(bfr(vb + 1e-5f)));
#pragma unroll
    for (int i = 0; i < 8; ++i) x[i] = bfr(bfr(x[i] - mb) / den);
}
__device__ __forceinline__ void unpack8(u32x4 w, float* x) {
#pragma unroll
    for (int i = 0; i < 4; ++i) { x[2 * i] = __uint_as_float(w[i] << 16); x[2 * i + 1] = __uint_as_float(w[i] & 0xffff0000u); }
}
__device__ __forceinline__ u32x4 pack8f(const float* x) {
    u32x4 w = {cvtpk(x[0], x[1]), cvtpk(x[2], x[3]), cvtpk(x[4], x[5]), cvtpk(x[6], x[7])};
    return w;
}

__global__ __launch_bounds__(256) void ln1_kernel(const u16* __restrict__ x, u16* __restrict__ xn) {
    const int row = blockIdx.x * 4 + (threadIdx.x >> 6), lane = threadIdx.x & 63;
    u32x4 w = *(const u32x4*)(x + (size_t)row * DM + lane * 8);
    float v[8]; unpack8(w, v); ln8(v);
    *(u32x4*)(xn + (size_t)row * DM + lane * 8) = pack8f(v);
}

template <int KS>
__global__ __launch_bounds__(256) void oreduce_ln2_kernel(const float* __restrict__ part, const u16* __restrict__ x,
                                                          u16* __restrict__ h, u16* __restrict__ hn) {
    const int row = blockIdx.x * 4 + (threadIdx.x >> 6), lane = threadIdx.x & 63;
    float a[8];
#pragma unroll
    for (int i = 0; i < 8; ++i) a[i] = 0.f;
#pragma unroll
    for (int s = 0; s < KS; ++s) {
        const float* p = part + ((size_t)s * SEQ + row) * DM + lane * 8;
        f32x4 p0 = *(const f32x4*)p, p1 = *(const f32x4*)(p + 4);
#pragma unroll
        for (int i = 0; i < 4; ++i) { a[i] += p0[i]; a[4 + i] += p1[i]; }
    }
    u32x4 w = *(const u32x4*)(x + (size_t)row * DM + lane * 8);
    float v[8]; unpack8(w, v);
#pragma unroll
    for (int i = 0; i < 8; ++i) v[i] = bfr(v[i] + bfr(a[i]));
    *(u32x4*)(h + (size_t)row * DM + lane * 8) = pack8f(v);
    ln8(v);
    *(u32x4*)(hn + (size_t)row * DM + lane * 8) = pack8f(v);
}

enum { EPI_QKV = 0, EPI_PART = 1, EPI_GELU = 2, EPI_FFN2 = 3 };
struct GemmArgs {
    const u16* A[3]; const u16* B[3]; void* C[3];
    const u16* bias; const u16* res;
    int lda, ldb, M, N, K, Kv, nTn, ksplit;
};

__device__ __forceinline__ int a_lds_byte(int r, int c) {
    const int st = (r >> 4) * 2 + (c >> 5), ob = (r & 15) * 64 + (c & 31) * 2;
    return st * 1024 + (ob ^ (((ob >> 9) & 1) << 5));
}
__device__ __forceinline__ void a_stage_rc(int b, int& R, int& C) {
    const int st = b >> 10, sb = b & 1023, swz = sb ^ (((sb >> 9) & 1) << 5);
    R = (st >> 1) * 16 + (swz >> 6); C = (st & 1) * 32 + ((swz & 63) >> 1);
}

template <int BM, int BN, int WM, int WN, int EPI>
__global__ __launch_bounds__(WM * WN * 64) void gemm_kernel(GemmArgs g) {
    constexpr int NT = WM * WN * 64, TM = BM / WM, TN = BN / WN, MI = TM / 16, NI = TN / 16;
    constexpr int A_BYTES = BM * 64 * 2, B_BYTES = BN * 64 * 2, NSUB = BN / 32;
    extern __shared__ __attribute__((aligned(16))) char smem[];
    const int tid = threadIdx.x, wid = tid >> 6, lane = tid & 63, wm = wid / WN, wn = wid % WN;
    const int nkt = g.K / 64, tilesPerZ = g.nTn * (g.M / BM), ntiles = tilesPerZ * g.ksplit;
    int ld_t = blockIdx.x, ld_kt = 0;
    auto stage = [&](int buf) {
        if (ld_t >= ntiles) return;
        const int z = ld_t / tilesPerZ, rem = ld_t - z * tilesPerZ, tn = rem % g.nTn, tm = rem / g.nTn;
        const int mat = (EPI == EPI_QKV) ? z : 0, ksp = (EPI == EPI_PART) ? z : 0;
        const u16* A = mat == 0 ? g.A[0] : (mat == 1 ? g.A[1] : g.A[2]); const u16* B = mat == 0 ? g.B[0] : (mat == 1 ? g.B[1] : g.B[2]);
        const int m0 = tm * BM, n0 = tn * BN, k0 = ksp * g.K + ld_kt * 64;
        char* sa = smem + buf * (A_BYTES + B_BYTES); char* sb = sa + A_BYTES;
#pragma unroll
        for (int i = 0; i < A_BYTES / (NT * 16); ++i) {
            const int b = (i * NT + tid) * 16; int R, C; a_stage_rc(b, R, C);
            __builtin_amdgcn_global_load_lds((const unsigned*)(A + (size_t)(m0 + R) * g.lda + k0 + C), (unsigned*)(sa + b), 16, 0, 0);
        }
#pragma unroll
        for (int i = 0; i < B_BYTES / (NT * 16); ++i) {
            const int P = i * NT + tid, s = P >> 5, r7 = (P >> 2) & 7, cx = P & 3;
            const int row = 8 * (s / NSUB) + r7, ch = 4 * (s % NSUB) + (cx ^ ((row >> 2) & 3));
            int kr = k0 + row; if (kr > g.Kv - 1) kr = g.Kv - 1;
            int nc = n0 + ch * 8; if (nc > g.N - 8) nc = g.N - 8;
            __builtin_amdgcn_global_load_lds((const unsigned*)(B + (size_t)kr * g.ldb + nc), (unsigned*)(sb + P * 16), 16, 0, 0);
        }
        if (++ld_kt == nkt) { ld_kt = 0; ld_t += gridDim.x; }
    };

    f32x4 acc[MI][NI];
#pragma unroll
    for (int i = 0; i < MI; ++i)
#pragma unroll
        for (int j = 0; j < NI; ++j) acc[i][j] = f32x4{0.f, 0.f, 0.f, 0.f};

    const int g4 = lane >> 4, i16 = lane & 15, q4 = i16 >> 2, p4 = i16 & 3;
    const int a_base = a_lds_byte(i16, 8 * g4);
    int b_base[2][2];
#pragma unroll
    for (int t = 0; t < 2; ++t)
#pragma unroll
        for (int par = 0; par < 2; ++par) {
            const int row = 8 * g4 + 4 * t + q4, chl = 2 * par + (p4 >> 1);
            b_base[t][par] = 512 * (g4 * NSUB + (wn * TN >> 5)) + 64 * (4 * t + q4) + 16 * (chl ^ ((row >> 2) & 3)) + 8 * (p4 & 1);
        }

    constexpr int LPS = (A_BYTES + B_BYTES) / (NT * 16);
    stage(0); stage(1);
    int cur = 0;
    for (int t = blockIdx.x; t < ntiles; t += gridDim.x) {
    const int z = t / tilesPerZ, rem_ = t - z * tilesPerZ, tn = rem_ % g.nTn, tm = rem_ / g.nTn;
    const int mat = (EPI == EPI_QKV) ? z : 0, ksp = (EPI == EPI_PART) ? z : 0;
    const int m0 = tm * BM, n0 = tn * BN;
    for (int kt = 0; kt < nkt; ++kt) {
        if (ld_t < ntiles) asm volatile("s_waitcnt vmcnt(%0)" :: "i"(LPS) : "memory");
        else asm volatile("s_waitcnt vmcnt(0)" ::: "memory");
        __builtin_amdgcn_s_barrier();
        stage(cur >= 1 ? cur - 1 : 2);
        const char* sa = smem + cur * (A_BYTES + B_BYTES);
        cur = cur == 2 ? 0 : cur + 1;
        const unsigned sbo = (unsigned)(uintptr_t)(sa + A_BYTES);
#pragma unroll
        for (int ks = 0; ks < 2; ++ks) {
            bf16x8 af[MI], bfr_[NI];
#pragma unroll
            for (int mi = 0; mi < MI; ++mi) af[mi] = *(const bf16x8*)(sa + a_base + ((wm * MI + mi) * 2 + ks) * 1024);
#pragma unroll
            for (int ni = 0; ni < NI; ++ni) {
                const int off = 512 * (4 * ks * NSUB + (ni >> 1));
                s16x4 lo, hi;
                const unsigned a0 = sbo + b_base[0][ni & 1], a1 = sbo + b_base[1][ni & 1];
                asm volatile("ds_read_b64_tr_b16 %0, %1 offset:%2" : "=&v"(lo) : "v"(a0), "i"(off) : "memory");
                asm volatile("ds_read_b64_tr_b16 %0, %1 offset:%2" : "=&v"(hi) : "v"(a1), "i"(off) : "memory");
                bfr_[ni] = (bf16x8){lo[0], lo[1], lo[2], lo[3], hi[0], hi[1], hi[2], hi[3]};
            }
            asm volatile("s_waitcnt lgkmcnt(0)" ::: "memory"); SBAR();
#pragma unroll
            for (int mi = 0; mi < MI; ++mi)
#pragma unroll
                for (int ni = 0; ni < NI; ++ni)
                    acc[mi][ni] = __builtin_amdgcn_mfma_f32_16x16x32_bf16(bfr_[ni], af[mi], acc[mi][ni], 0, 0, 0);
        }
    }
#pragma unroll
    for (int mi = 0; mi < MI; ++mi) {
        const int m = m0 + wm * TM + 16 * mi + i16;
        char* ep = smem + 3 * (A_BYTES + B_BYTES) + wid * (16 * 128);
#pragma unroll
        for (int ni = 0; ni < NI; ++ni) {
            const int n = n0 + wn * TN + 16 * ni + 4 * g4;
            f32x4 v = acc[mi][ni];
            if constexpr (EPI == EPI_QKV) {
                u32x2 w = {cvtpk(v[0], v[1]), cvtpk(v[2], v[3])};
                *(u32x2*)(ep + i16 * 128 + (((2 * ni + (g4 >> 1)) ^ (i16 & 7)) << 4) + (g4 & 1) * 8) = w;
            } else if constexpr (EPI == EPI_PART) {
                *(f32x4*)((float*)g.C[0] + ((size_t)ksp * g.M + m) * g.N + n) = v;
            } else if constexpr (EPI == EPI_GELU) {
                float o[4];
                if (n < g.N) {
                    u32x2 bw = *(const u32x2*)(g.bias + n);
                    const float b[4] = {__uint_as_float(bw[0] << 16), __uint_as_float(bw[0] & 0xffff0000u),
                                        __uint_as_float(bw[1] << 16), __uint_as_float(bw[1] & 0xffff0000u)};
#pragma unroll
                    for (int j = 0; j < 4; ++j) {
                        const float xx = bfr(bfr(v[j]) + b[j]);
                        const float u = 0.7978845608028654f * (xx + 0.044715f * xx * xx * xx);
                        const float th = 1.f - 2.f / (__expf(2.f * u) + 1.f);
                        o[j] = 0.5f * xx * (1.f + th);
                    }
                } else { o[0] = o[1] = o[2] = o[3] = 0.f; }
                if (n < DFFP) {
                    u32x2 w = {cvtpk(o[0], o[1]), cvtpk(o[2], o[3])};
                    *(u32x2*)((u16*)g.C[0] + (size_t)m * DFFP + n) = w;
                }
            } else {
                u32x2 bw = *(const u32x2*)(g.bias + n);
                u32x2 rw = *(const u32x2*)(g.res + (size_t)m * DM + n);
                const float b[4] = {__uint_as_float(bw[0] << 16), __uint_as_float(bw[0] & 0xffff0000u),
                                    __uint_as_float(bw[1] << 16), __uint_as_float(bw[1] & 0xffff0000u)};
                const float r[4] = {__uint_as_float(rw[0] << 16), __uint_as_float(rw[0] & 0xffff0000u),
                                    __uint_as_float(rw[1] << 16), __uint_as_float(rw[1] & 0xffff0000u)};
                float o[4];
#pragma unroll
                for (int j = 0; j < 4; ++j) o[j] = r[j] + bfr(bfr(v[j]) + b[j]);
                u32x2 w = {cvtpk(o[0], o[1]), cvtpk(o[2], o[3])};
                *(u32x2*)((u16*)g.C[0] + (size_t)m * DM + n) = w;
            }
            acc[mi][ni] = f32x4{0.f, 0.f, 0.f, 0.f};
        }
        if constexpr (EPI == EPI_QKV) {
            u16* C = (u16*)(mat == 0 ? g.C[0] : (mat == 1 ? g.C[1] : g.C[2]));
            asm volatile("s_waitcnt lgkmcnt(0)" ::: "memory");
#pragma unroll
            for (int it = 0; it < TN / 32; ++it) {
                const int idx = it * 64 + lane, rr = idx / (TN / 8), cc = idx % (TN / 8);
                u32x4 w = *(const u32x4*)(ep + rr * 128 + ((cc ^ (rr & 7)) << 4));
                const int mm = m0 + wm * TM + 16 * mi + rr, nn = n0 + wn * TN + cc * 8;
                *(u32x4*)(C + ((size_t)(nn >> 7) * SEQ + mm) * HD + (nn & 127)) = w;
            }
            asm volatile("s_waitcnt lgkmcnt(0)" ::: "memory");
        }
    }
    }
}

constexpr float SCALE = 0.08838834764831845f;
constexpr int ATT_LDS = 4 * 32768 + 8 * 64 * 4;
__device__ __forceinline__ int crow(int r, int hi) { return (r & 3) + 8 * (r >> 2) + 4 * hi; }
__device__ __forceinline__ int v_rd_base(int lane) { return ((lane & 3) << 3) | (((lane >> 2) & 3) << 6) | (((lane >> 4) & 1) << 5) | (((lane >> 5) & 1) << 8); }

__global__ __launch_bounds__(512, 1) void attn_kernel(const u16* __restrict__ Q, const u16* __restrict__ K, const u16* __restrict__ V,
                                                      u16* __restrict__ ctx) {
    extern __shared__ __attribute__((aligned(16))) char lds[];
    const int tid = threadIdx.x, wid = __builtin_amdgcn_readfirstlane(tid >> 6);
    const int rg = wid & 3, par = wid >> 2;
    const int h = (blockIdx.x & 7) + 8 * (blockIdx.x >> 6), xq = (blockIdx.x >> 3) & 7;
    const u16* Kh = K + (size_t)h * SEQ * HD; const u16* Vh = V + (size_t)h * SEQ * HD;
    char* K_lds = lds; char* V_lds = lds + 65536;
    float* al_l = (float*)(lds + 4 * 32768) + wid * 64;

    unsigned koff, voff, loff;
    auto stageK = [&](int s, int buf) {
        const char* kb_ = (const char*)Kh + (size_t)s * 32768;
#pragma unroll
        for (int i = 0; i < 4; ++i)
            __builtin_amdgcn_global_load_lds((const unsigned*)(kb_ + koff + i * 8192), (unsigned*)(K_lds + buf * 32768 + i * 8192 + loff), 16, 0, 0);
    };
    auto stageV = [&](int s, int buf) {
        const char* vb_ = (const char*)Vh + (size_t)s * 32768;
#pragma unroll
        for (int i = 0; i < 4; ++i)
            __builtin_amdgcn_global_load_lds((const unsigned*)(vb_ + voff + i * 8192), (unsigned*)(V_lds + buf * 32768 + i * 8192 + loff), 16, 0, 0);
    };
    constexpr float C2 = 1.4426950408889634f * SCALE;
#define QKT(P0, P1, buf, act) do { { \
        P0 = f32x16{}; P1 = f32x16{}; const char* kb[4]; \
        _Pragma("unroll") for (int dd = 0; dd < 4; ++dd) kb[dd] = K_lds + (buf) * 32768 + (par * 64 + r32) * 256 + (((dd * 16 + hi * 8) * 2) ^ ((r32 & 7) << 4)); \
        _Pragma("unroll") for (int d0 = 0; d0 < 8; ++d0) { const char* a_ = kb[d0 & 3] + (d0 >> 2) * 128; \
            bf16x8 b0 = *(const bf16x8*)a_, b1 = *(const bf16x8*)(a_ + 32 * 256); \
            P0 = __builtin_amdgcn_mfma_f32_32x32x16_bf16(b0, qr[d0], P0, 0, 0, 0); \
            P1 = __builtin_amdgcn_mfma_f32_32x32x16_bf16(b1, qr[d0], P1, 0, 0, 0); } } } while (0)
#define PARTSM(P0, P1, s_, alpha) do { \
        const int kbase_ = 128 * (s_) + 64 * par; \
        if (kbase_ + 63 > qrow0) { const int dq = qrow0 + r32 - kbase_ - 4 * hi; const float NEG_ = -__builtin_inff(); \
            _Pragma("unroll") for (int r = 0; r < 16; ++r) { const int c = (r & 3) + 8 * (r >> 2); \
                if (dq - c < 0) P0[r] = NEG_; if (dq - c - 32 < 0) P1[r] = NEG_; } } \
        float pmax = P0[0]; \
        _Pragma("unroll") for (int r = 1; r < 16; ++r) pmax = fmaxf(pmax, P0[r]); \
        _Pragma("unroll") for (int r = 0; r < 16; ++r) pmax = fmaxf(pmax, P1[r]); \
        { auto rr = __builtin_amdgcn_permlane32_swap(__float_as_uint(pmax), __float_as_uint(pmax), false, false); \
          pmax = fmaxf(__uint_as_float(rr[0]), __uint_as_float(rr[1])); } \
          \
        if (__builtin_expect(__all((pmax - m_reg) * SCALE <= 8.f), 1)) { alpha = 1.f; } \
        else { const float mn = fmaxf(m_reg, pmax); alpha = __builtin_amdgcn_exp2f((m_reg - mn) * C2); m_reg = mn; } \
        const float mnL = -m_reg * C2; \
        _Pragma("unroll") for (int r = 0; r < 16; ++r) { P0[r] = __builtin_amdgcn_exp2f(fmaf(P0[r], C2, mnL)); P1[r] = fmaf(P1[r], C2, mnL); } } while (0)
#define PK4(P, B_, OUT) do { unsigned a0 = cvtpk(P[B_+0], P[B_+1]), a1 = cvtpk(P[B_+2], P[B_+3]);                          \
        unsigned b0 = cvtpk(P[B_+4], P[B_+5]), b1 = cvtpk(P[B_+6], P[B_+7]);                                             \
        auto r0 = __builtin_amdgcn_permlane32_swap(a0, b0, false, false); auto r1 = __builtin_amdgcn_permlane32_swap(a1, b1, false, false); \
        u32x4 w = {r0[0], r1[0], r0[1], r1[1]}; OUT = *reinterpret_cast<bf16x8*>(&w); } while (0)
#define FINSM(P0, P1, alpha) do { float ps = 0.f; \
        _Pragma("unroll") for (int r = 0; r < 16; ++r) { P1[r] = __builtin_amdgcn_exp2f(P1[r]); ps += P0[r]; } \
        _Pragma("unroll") for (int r = 0; r < 16; ++r) ps += P1[r]; \
        { auto rr = __builtin_amdgcn_permlane32_swap(__float_as_uint(ps), __float_as_uint(ps), false, false); \
          ps = __uint_as_float(rr[0]) + __uint_as_float(rr[1]); } \
        l_reg = l_reg * (alpha) + ps; \
        PK4(P0, 0, pa0); PK4(P0, 8, pa1); PK4(P1, 0, pa2); PK4(P1, 8, pa3); } while (0)
#define RESC(a) do { if (__any((a) < 1.f)) { if (hi == 0) al_l[r32] = (a); asm volatile("s_waitcnt lgkmcnt(0)" ::: "memory"); \
        _Pragma("unroll") for (int d_ = 0; d_ < 4; ++d_) _Pragma("unroll") for (int r = 0; r < 16; ++r) o[d_][r] *= al_l[crow(r, hi)]; } } while (0)
#define TRRD(dst, off) asm volatile("ds_read_b64_tr_b16 %0, %1 offset:%2" : "=&v"(dst) : "v"(vb), "i"(off) : "memory")
#define PV_D0(d0) do { s16x4 l0, l1, l2, l3, h0, h1, h2, h3; constexpr int b_ = (d0) * 512; \
        TRRD(l0, b_); TRRD(h0, b_ + 2048); TRRD(l1, b_ + 4096); TRRD(h1, b_ + 6144); TRRD(l2, b_ + 8192); TRRD(h2, b_ + 10240); TRRD(l3, b_ + 12288); TRRD(h3, b_ + 14336); \
        asm volatile("s_waitcnt lgkmcnt(0)" ::: "memory"); SBAR(); \
        o[d0] = __builtin_amdgcn_mfma_f32_32x32x16_bf16(pa0, (bf16x8){l0[0], l0[1], l0[2], l0[3], h0[0], h0[1], h0[2], h0[3]}, o[d0], 0, 0, 0);   \
        o[d0] = __builtin_amdgcn_mfma_f32_32x32x16_bf16(pa1, (bf16x8){l1[0], l1[1], l1[2], l1[3], h1[0], h1[1], h1[2], h1[3]}, o[d0], 0, 0, 0);   \
        o[d0] = __builtin_amdgcn_mfma_f32_32x32x16_bf16(pa2, (bf16x8){l2[0], l2[1], l2[2], l2[3], h2[0], h2[1], h2[2], h2[3]}, o[d0], 0, 0, 0);   \
        o[d0] = __builtin_amdgcn_mfma_f32_32x32x16_bf16(pa3, (bf16x8){l3[0], l3[1], l3[2], l3[3], h3[0], h3[1], h3[2], h3[3]}, o[d0], 0, 0, 0); } while (0)
#define PV(buf) do { const int vb = vb0 + (buf) * 32768; PV_D0(0); PV_D0(1); PV_D0(2); PV_D0(3); } while (0)
#define ACT(s_) (128 * (s_) + 64 * par <= qrow0 + 31)
#define ITER(PX0, PX1, alX, PY0, PY1, alY, s_, E) do { \
        asm volatile("s_waitcnt vmcnt(0)" ::: "memory"); __syncthreads(); \
        if ((s_) + 2 < nsteps) stageK((s_) + 2, E); \
        stageV((s_) + 1, 1 - (E)); SBAR(); \
        QKT(PX0, PX1, 1 - (E), ACT((s_) + 1)); \
        FINSM(PY0, PY1, alY); SBAR(); \
        PV(E); PARTSM(PX0, PX1, (s_) + 1, alX); \
        RESC(alX); } while (0)

    for (int pass = 0; pass < 2; ++pass) {
        const int qb = pass ? 15 - xq : xq, nsteps = qb + 1;
        const int qrow0 = qb * 128 + rg * 32;
        int tz = tid; asm volatile("" : "+v"(tz));
        int r32 = tz & 31, hi = (tz >> 5) & 1;
        const int vb0 = (int)(uintptr_t)V_lds + par * 16384 + v_rd_base(tz & 63);
        koff = (unsigned)((tz >> 4) * 256 + (((tz & 15) ^ ((tz >> 4) & 7)) << 4)); loff = (unsigned)tz * 16;
        { const int sub = tz >> 5, r7 = (tz >> 2) & 7, c8 = tz & 3, kk = (sub >> 2) * 8 + r7;
          const int k = (kk & ~0xC) | ((kk & 4) << 1) | ((kk & 8) >> 1);
          voff = (unsigned)(k * 256 + ((sub & 3) * 32 + c8 * 8) * 2); }
        bf16x8 qr[8];
#pragma unroll
        for (int d0 = 0; d0 < 8; ++d0) qr[d0] = *(const bf16x8*)(Q + ((size_t)h * SEQ + qrow0 + r32) * HD + d0 * 16 + hi * 8);
        float m_reg = -1e30f, l_reg = 0.f; f32x16 o[4] = {};
        f32x16 pA0, pA1, pB0, pB1; float alA, alB; bf16x8 pa0, pa1, pa2, pa3;
        __syncthreads();
        stageK(0, 0); stageV(0, 0);
        asm volatile("s_waitcnt vmcnt(0)" ::: "memory"); __syncthreads();
        if (nsteps > 1) stageK(1, 1);
        QKT(pA0, pA1, 0, ACT(0)); PARTSM(pA0, pA1, 0, alA);
        int s = 0;
        for (; s + 2 < nsteps; s += 2) {
            ITER(pB0, pB1, alB, pA0, pA1, alA, s, 0);
            ITER(pA0, pA1, alA, pB0, pB1, alB, s + 1, 1);
        }
        if (s + 1 < nsteps) {
            ITER(pB0, pB1, alB, pA0, pA1, alA, s, 0);
            asm volatile("s_waitcnt vmcnt(0)" ::: "memory"); __syncthreads();
            FINSM(pB0, pB1, alB); SBAR(); PV(1);
        } else {
            asm volatile("s_waitcnt vmcnt(0)" ::: "memory"); __syncthreads();
            FINSM(pA0, pA1, alA); SBAR(); PV(0);
        }
        __syncthreads();
        tz = tid; asm volatile("" : "+v"(tz)); r32 = tz & 31; hi = (tz >> 5) & 1;
        float* cO = (float*)lds + rg * (32 * 128 + 64);
        if (par == 1) {
#pragma unroll
            for (int d = 0; d < 4; ++d)
#pragma unroll
                for (int r = 0; r < 16; ++r) cO[crow(r, hi) * 128 + d * 32 + r32] = o[d][r];
            if (hi == 0) { cO[4096 + r32] = m_reg; cO[4096 + 32 + r32] = l_reg; }
        }
        __syncthreads();
        if (par == 0) {
            const float m1 = cO[4096 + r32], l1 = cO[4096 + 32 + r32];
            const float mm = fmaxf(m_reg, m1);
            const float a0 = __builtin_amdgcn_exp2f((m_reg - mm) * C2), a1 = __builtin_amdgcn_exp2f((m1 - mm) * C2);
            const float linv = 1.f / (l_reg * a0 + l1 * a1);
            if (hi == 0) { al_l[r32] = a0 * linv; al_l[32 + r32] = a1 * linv; }
            asm volatile("s_waitcnt lgkmcnt(0)" ::: "memory");
#pragma unroll
            for (int r = 0; r < 16; ++r) {
                const int orow = crow(r, hi); const float c0 = al_l[orow], c1 = al_l[32 + orow];
#pragma unroll
                for (int d = 0; d < 4; ++d) {
                    const float v = o[d][r] * c0 + cO[orow * 128 + d * 32 + r32] * c1;
                    const float vn = __shfl_xor(v, 1);
                    if ((r32 & 1) == 0) *(unsigned*)(ctx + (size_t)(qrow0 + orow) * (NH * HD) + h * HD + d * 32 + r32) = cvtpk(v, vn);
                }
            }
        }
    }
}

extern "C" void kernel_launch(void* const* d_in, const int* in_sizes, int n_in, void* d_out, int out_size, void* d_ws, size_t ws_size,
                              hipStream_t stream) {
    const u16* x = (const u16*)d_in[0]; const u16* ckv = (const u16*)d_in[1];
    const u16* wq = (const u16*)d_in[2]; const u16* wuk = (const u16*)d_in[3]; const u16* wuv = (const u16*)d_in[4]; const u16* wo = (const u16*)d_in[5];
    const u16* w1 = (const u16*)d_in[7]; const u16* b1 = (const u16*)d_in[8]; const u16* w2 = (const u16*)d_in[9]; const u16* b2 = (const u16*)d_in[10];
    u16* out = (u16*)d_out;
    char* ws = (char*)d_ws;
    u16* xn = (u16*)ws;                                   ws += (size_t)SEQ * DM * 2;
    u16* qb = (u16*)ws;                                   ws += (size_t)SEQ * NH * HD * 2;
    u16* kb = (u16*)ws;                                   ws += (size_t)SEQ * NH * HD * 2;
    u16* vb = (u16*)ws;                                   ws += (size_t)SEQ * NH * HD * 2;
    u16* ctx = (u16*)ws;                                  ws += (size_t)SEQ * NH * HD * 2;
    u16* hbuf = (u16*)ws;                                 ws += (size_t)SEQ * DM * 2;
    u16* hn = (u16*)ws;                                   ws += (size_t)SEQ * DM * 2;
    u16* fbuf = (u16*)ws;                                 ws += (size_t)SEQ * DFFP * 2;
    float* part = (float*)ws;                             ws += (size_t)4 * SEQ * DM * 4;

    constexpr int KS = 4;
    auto gq = gemm_kernel<128, 256, 2, 4, EPI_QKV>;
    auto go = gemm_kernel<128, 128, 2, 4, EPI_PART>;
    auto g1 = gemm_kernel<128, 128, 2, 4, EPI_GELU>;
    auto g2 = gemm_kernel<128, 64, 4, 2, EPI_FFN2>;
    (void)hipFuncSetAttribute((const void*)gq, hipFuncAttributeMaxDynamicSharedMemorySize, 3 * (128 + 256) * 128 + 8 * 16 * 128);
    (void)hipFuncSetAttribute((const void*)go, hipFuncAttributeMaxDynamicSharedMemorySize, 3 * (128 + 128) * 128);
    (void)hipFuncSetAttribute((const void*)g1, hipFuncAttributeMaxDynamicSharedMemorySize, 3 * (128 + 128) * 128);
    (void)hipFuncSetAttribute((const void*)g2, hipFuncAttributeMaxDynamicSharedMemorySize, 3 * (128 + 64) * 128);
    (void)hipFuncSetAttribute((const void*)attn_kernel, hipFuncAttributeMaxDynamicSharedMemorySize, ATT_LDS);

    ln1_kernel<<<SEQ / 4, 256, 0, stream>>>(x, xn);
    {
        GemmArgs g = {};
        g.A[0] = xn; g.A[1] = ckv; g.A[2] = ckv; g.B[0] = wq; g.B[1] = wuk; g.B[2] = wuv; g.C[0] = qb; g.C[1] = kb; g.C[2] = vb;
        g.lda = DM; g.ldb = NH * HD; g.M = SEQ; g.N = NH * HD; g.K = DM; g.Kv = DM; g.ksplit = 1;
        g.nTn = (NH * HD) / 256;
        g.ksplit = 3; gq<<<256, 512, 3 * (128 + 256) * 128 + 8 * 16 * 128, stream>>>(g);
    }
    attn_kernel<<<NH * 8, 512, ATT_LDS, stream>>>(qb, kb, vb, ctx);
    {
        GemmArgs g = {};
        g.A[0] = ctx; g.B[0] = wo; g.C[0] = part; g.lda = NH * HD; g.ldb = DM; g.M = SEQ; g.N = DM; g.K = NH * HD / KS; g.Kv = NH * HD;
        g.nTn = DM / 128; g.ksplit = KS;
        go<<<256, 512, 3 * (128 + 128) * 128, stream>>>(g);
    }
    oreduce_ln2_kernel<KS><<<SEQ / 4, 256, 0, stream>>>(part, x, hbuf, hn);
    {
        GemmArgs g = {};
        g.A[0] = hn; g.B[0] = w1; g.C[0] = fbuf; g.bias = b1; g.lda = DM; g.ldb = DFF; g.M = SEQ; g.N = DFF; g.K = DM; g.Kv = DM;
        g.nTn = (DFF + 127) / 128; g.ksplit = 1;
        g1<<<176, 512, 3 * (128 + 128) * 128, stream>>>(g);
    }
    {
        GemmArgs g = {};
        g.A[0] = fbuf; g.B[0] = w2; g.C[0] = out; g.bias = b2; g.res = hbuf; g.lda = DFFP; g.ldb = DM; g.M = SEQ; g.N = DM; g.K = DFFP; g.Kv = DFF;
        g.nTn = DM / 64; g.ksplit = 1;
        g2<<<128, 512, 3 * (128 + 64) * 128, stream>>>(g);
    }
}
```

```cpp
#include <hip/hip_runtime.h>
#include <hip/hip_bf16.h>
#include <stdint.h>

typedef short bf16x8 __attribute__((ext_vector_type(8)));
typedef short s16x4 __attribute__((ext_vector_type(4)));
typedef float f32x4 __attribute__((ext_vector_type(4)));
typedef float f32x16 __attribute__((ext_vector_type(16)));
typedef unsigned u32x4 __attribute__((ext_vector_type(4)));
typedef unsigned u32x2 __attribute__((ext_vector_type(2)));
typedef unsigned short u16;

#define SBAR() __builtin_amdgcn_sched_barrier(0)

constexpr int SEQ = 2048, DM = 512, NH = 32, HD = 128, DC = 512, DFF = 1376, DFFP = 1408;

__device__ __forceinline__ unsigned cvtpk(float lo, float hi) {
    typedef float f32x2_ __attribute__((ext_vector_type(2))); typedef __bf16 bf16x2_ __attribute__((ext_vector_type(2)));
    f32x2_ v = {lo, hi}; bf16x2_ b = __builtin_convertvector(v, bf16x2_);
    return __builtin_bit_cast(unsigned, b);
}
__device__ __forceinline__ float bf2f(u16 v) { return __uint_as_float(((unsigned)v) << 16); }
__device__ __forceinline__ float bfr(float v) { return __uint_as_float(cvtpk(v, v) << 16); }

__device__ __forceinline__ float wave_sum(float v) {
#pragma unroll
    for (int o = 32; o > 0; o >>= 1) v += __shfl_xor(v, o);
    return v;
}
__device__ __forceinline__ void ln8(float* x) {
    float s = 0;
#pragma unroll
    for (int i = 0; i < 8; ++i) s += x[i];
    s = wave_sum(s);
    const float mean = s * (1.f / 512.f);
    float q = 0;
#pragma unroll
    for (int i = 0; i < 8; ++i) { const float d = x[i] - mean; q += d * d; }
    q = wave_sum(q);
    const float mb = bfr(mean);
    const float vb = bfr(q * (1.f / 512.f));
    const float den = bfr(sqrtf(bfr(vb + 1e-5f)));
#pragma unroll
    for (int i = 0; i < 8; ++i) x[i] = bfr(bfr(x[i] - mb) / den);
}
__device__ __forceinline__ void unpack8(u32x4 w, float* x) {
#pragma unroll
    for (int i = 0; i < 4; ++i) { x[2 * i] = __uint_as_float(w[i] << 16); x[2 * i + 1] = __uint_as_float(w[i] & 0xffff0000u); }
}
__device__ __forceinline__ u32x4 pack8f(const float* x) {
    u32x4 w = {cvtpk(x[0], x[1]), cvtpk(x[2], x[3]), cvtpk(x[4], x[5]), cvtpk(x[6], x[7])};
    return w;
}

__global__ __launch_bounds__(256) void ln1_kernel(const u16* __restrict__ x, u16* __restrict__ xn) {
    const int row = blockIdx.x * 4 + (threadIdx.x >> 6), lane = threadIdx.x & 63;
    u32x4 w = *(const u32x4*)(x + (size_t)row * DM + lane * 8);
    float v[8]; unpack8(w, v); ln8(v);
    *(u32x4*)(xn + (size_t)row * DM + lane * 8) = pack8f(v);
}

template <int KS>
__global__ __launch_bounds__(256) void oreduce_ln2_kernel(const float* __restrict__ part, const u16* __restrict__ x,
                                                          u16* __restrict__ h, u16* __restrict__ hn) {
    const int row = blockIdx.x * 4 + (threadIdx.x >> 6), lane = threadIdx.x & 63;
    float a[8];
#pragma unroll
    for (int i = 0; i < 8; ++i) a[i] = 0.f;
#pragma unroll
    for (int s = 0; s < KS; ++s) {
        const float* p = part + ((size_t)s * SEQ + row) * DM + lane * 8;
        f32x4 p0 = *(const f32x4*)p, p1 = *(const f32x4*)(p + 4);
#pragma unroll
        for (int i = 0; i < 4; ++i) { a[i] += p0[i]; a[4 + i] += p1[i]; }
    }
    u32x4 w = *(const u32x4*)(x + (size_t)row * DM + lane * 8);
    float v[8]; unpack8(w, v);
#pragma unroll
    for (int i = 0; i < 8; ++i) v[i] = bfr(v[i] + bfr(a[i]));
    *(u32x4*)(h + (size_t)row * DM + lane * 8) = pack8f(v);
    ln8(v);
    *(u32x4*)(hn + (size_t)row * DM + lane * 8) = pack8f(v);
}

enum { EPI_QKV = 0, EPI_PART = 1, EPI_GELU = 2, EPI_FFN2 = 3 };
struct GemmArgs {
    const u16* A[3]; const u16* B[3]; void* C[3];
    const u16* bias; const u16* res;
    int lda, ldb, M, N, K, Kv, nTn, ksplit;
};

__device__ __forceinline__ int a_lds_byte(int r, int c) {
    const int st = (r >> 4) * 2 + (c >> 5), ob = (r & 15) * 64 + (c & 31) * 2;
    return st * 1024 + (ob ^ (((ob >> 9) & 1) << 5));
}
__device__ __forceinline__ void a_stage_rc(int b, int& R, int& C) {
    const int st = b >> 10, sb = b & 1023, swz = sb ^ (((sb >> 9) & 1) << 5);
    R = (st >> 1) * 16 + (swz >> 6); C = (st & 1) * 32 + ((swz & 63) >> 1);
}

template <int BM, int BN, int WM, int WN, int EPI, int NST>
__global__ __launch_bounds__(WM * WN * 64) void gemm_kernel(GemmArgs g) {
    constexpr int NT = WM * WN * 64, TM = BM / WM, TN = BN / WN, MI = TM / 16, NI = TN / 16;
    constexpr int A_BYTES = BM * 64 * 2, B_BYTES = BN * 64 * 2, NSUB = BN / 32;
    extern __shared__ __attribute__((aligned(16))) char smem[];
    const int tid = threadIdx.x, wid = tid >> 6, lane = tid & 63, wm = wid / WN, wn = wid % WN;
    const int nkt = g.K / 64, tilesPerZ = g.nTn * (g.M / BM), ntiles = tilesPerZ * g.ksplit;
    int ld_t = blockIdx.x, ld_kt = 0;
    auto stage = [&](int buf) {
        if (ld_t >= ntiles) return;
        const int z = ld_t / tilesPerZ, rem = ld_t - z * tilesPerZ, tn = rem % g.nTn, tm = rem / g.nTn;
        const int mat = (EPI == EPI_QKV) ? z : 0, ksp = (EPI == EPI_PART) ? z : 0;
        const u16* A = mat == 0 ? g.A[0] : (mat == 1 ? g.A[1] : g.A[2]); const u16* B = mat == 0 ? g.B[0] : (mat == 1 ? g.B[1] : g.B[2]);
        const int m0 = tm * BM, n0 = tn * BN, k0 = ksp * g.K + ld_kt * 64;
        char* sa = smem + buf * (A_BYTES + B_BYTES); char* sb = sa + A_BYTES;
#pragma unroll
        for (int i = 0; i < A_BYTES / (NT * 16); ++i) {
            const int b = (i * NT + tid) * 16; int R, C; a_stage_rc(b, R, C);
            __builtin_amdgcn_global_load_lds((const unsigned*)(A + (size_t)(m0 + R) * g.lda + k0 + C), (unsigned*)(sa + b), 16, 0, 0);
        }
#pragma unroll
        for (int i = 0; i < B_BYTES / (NT * 16); ++i) {
            const int P = i * NT + tid, s = P >> 5, r7 = (P >> 2) & 7, cx = P & 3;
            const int row = 8 * (s / NSUB) + r7, ch = 4 * (s % NSUB) + (cx ^ ((row >> 2) & 3));
            int kr = k0 + row; if (kr > g.Kv - 1) kr = g.Kv - 1;
            int nc = n0 + ch * 8; if (nc > g.N - 8) nc = g.N - 8;
            __builtin_amdgcn_global_load_lds((const unsigned*)(B + (size_t)kr * g.ldb + nc), (unsigned*)(sb + P * 16), 16, 0, 0);
        }
        if (++ld_kt == nkt) { ld_kt = 0; ld_t += gridDim.x; }
    };

    f32x4 acc[MI][NI];
#pragma unroll
    for (int i = 0; i < MI; ++i)
#pragma unroll
        for (int j = 0; j < NI; ++j) acc[i][j] = f32x4{0.f, 0.f, 0.f, 0.f};

    const int g4 = lane >> 4, i16 = lane & 15, q4 = i16 >> 2, p4 = i16 & 3;
    const int a_base = a_lds_byte(i16, 8 * g4);
    int b_base[2][2];
#pragma unroll
    for (int t = 0; t < 2; ++t)
#pragma unroll
        for (int par = 0; par < 2; ++par) {
            const int row = 8 * g4 + 4 * t + q4, chl = 2 * par + (p4 >> 1);
            b_base[t][par] = 512 * (g4 * NSUB + (wn * TN >> 5)) + 64 * (4 * t + q4) + 16 * (chl ^ ((row >> 2) & 3)) + 8 * (p4 & 1);
        }

    constexpr int LPS = (A_BYTES + B_BYTES) / (NT * 16);
#pragma unroll
    for (int i = 0; i < NST - 1; ++i) stage(i);
    int cur = 0;
    for (int t = blockIdx.x; t < ntiles; t += gridDim.x) {
    const int z = t / tilesPerZ, rem_ = t - z * tilesPerZ, tn = rem_ % g.nTn, tm = rem_ / g.nTn;
    const int mat = (EPI == EPI_QKV) ? z : 0, ksp = (EPI == EPI_PART) ? z : 0;
    const int m0 = tm * BM, n0 = tn * BN;
    for (int kt = 0; kt < nkt; ++kt) {
        if (ld_t < ntiles) asm volatile("s_waitcnt vmcnt(%0)" :: "i"(LPS * (NST - 2)) : "memory");
        else asm volatile("s_waitcnt vmcnt(0)" ::: "memory");
        __builtin_amdgcn_s_barrier();
        stage(cur >= 1 ? cur - 1 : NST - 1);
        const char* sa = smem + cur * (A_BYTES + B_BYTES);
        cur = cur == NST - 1 ? 0 : cur + 1;
        const unsigned sbo = (unsigned)(uintptr_t)(sa + A_BYTES);
#pragma unroll
        for (int ks = 0; ks < 2; ++ks) {
            bf16x8 af[MI], bfr_[NI];
#pragma unroll
            for (int mi = 0; mi < MI; ++mi) af[mi] = *(const bf16x8*)(sa + a_base + ((wm * MI + mi) * 2 + ks) * 1024);
#pragma unroll
            for (int ni = 0; ni < NI; ++ni) {
                const int off = 512 * (4 * ks * NSUB + (ni >> 1));
                s16x4 lo, hi;
                const unsigned a0 = sbo + b_base[0][ni & 1], a1 = sbo + b_base[1][ni & 1];
                asm volatile("ds_read_b64_tr_b16 %0, %1 offset:%2" : "=&v"(lo) : "v"(a0), "i"(off) : "memory");
                asm volatile("ds_read_b64_tr_b16 %0, %1 offset:%2" : "=&v"(hi) : "v"(a1), "i"(off) : "memory");
                bfr_[ni] = (bf16x8){lo[0], lo[1], lo[2], lo[3], hi[0], hi[1], hi[2], hi[3]};
            }
            asm volatile("s_waitcnt lgkmcnt(0)" ::: "memory"); SBAR();
#pragma unroll
            for (int mi = 0; mi < MI; ++mi)
#pragma unroll
                for (int ni = 0; ni < NI; ++ni)
                    acc[mi][ni] = __builtin_amdgcn_mfma_f32_16x16x32_bf16(bfr_[ni], af[mi], acc[mi][ni], 0, 0, 0);
        }
    }
#pragma unroll
    for (int mi = 0; mi < MI; ++mi) {
        const int m = m0 + wm * TM + 16 * mi + i16;
        char* ep = smem + NST * (A_BYTES + B_BYTES) + wid * (16 * 128);
#pragma unroll
        for (int ni = 0; ni < NI; ++ni) {
            const int n = n0 + wn * TN + 16 * ni + 4 * g4;
            f32x4 v = acc[mi][ni];
            if constexpr (EPI == EPI_QKV) {
                u32x2 w = {cvtpk(v[0], v[1]), cvtpk(v[2], v[3])};
                *(u32x2*)(ep + i16 * 128 + (((2 * ni + (g4 >> 1)) ^ (i16 & 7)) << 4) + (g4 & 1) * 8) = w;
            } else if constexpr (EPI == EPI_PART) {
                *(f32x4*)((float*)g.C[0] + ((size_t)ksp * g.M + m) * g.N + n) = v;
            } else if constexpr (EPI == EPI_GELU) {
                float o[4];
                if (n < g.N) {
                    u32x2 bw = *(const u32x2*)(g.bias + n);
                    const float b[4] = {__uint_as_float(bw[0] << 16), __uint_as_float(bw[0] & 0xffff0000u),
                                        __uint_as_float(bw[1] << 16), __uint_as_float(bw[1] & 0xffff0000u)};
#pragma unroll
                    for (int j = 0; j < 4; ++j) {
                        const float xx = bfr(bfr(v[j]) + b[j]);
                        const float u = 0.7978845608028654f * (xx + 0.044715f * xx * xx * xx);
                        const float th = 1.f - 2.f / (__expf(2.f * u) + 1.f);
                        o[j] = 0.5f * xx * (1.f + th);
                    }
                } else { o[0] = o[1] = o[2] = o[3] = 0.f; }
                if (n < DFFP) {
                    u32x2 w = {cvtpk(o[0], o[1]), cvtpk(o[2], o[3])};
                    *(u32x2*)((u16*)g.C[0] + (size_t)m * DFFP + n) = w;
                }
            } else {
                u32x2 bw = *(const u32x2*)(g.bias + n);
                u32x2 rw = *(const u32x2*)(g.res + (size_t)m * DM + n);
                const float b[4] = {__uint_as_float(bw[0] << 16), __uint_as_float(bw[0] & 0xffff0000u),
                                    __uint_as_float(bw[1] << 16), __uint_as_float(bw[1] & 0xffff0000u)};
                const float r[4] = {__uint_as_float(rw[0] << 16), __uint_as_float(rw[0] & 0xffff0000u),
                                    __uint_as_float(rw[1] << 16), __uint_as_float(rw[1] & 0xffff0000u)};
                float o[4];
#pragma unroll
                for (int j = 0; j < 4; ++j) o[j] = r[j] + bfr(bfr(v[j]) + b[j]);
                u32x2 w = {cvtpk(o[0], o[1]), cvtpk(o[2], o[3])};
                *(u32x2*)((u16*)g.C[0] + (size_t)m * DM + n) = w;
            }
            acc[mi][ni] = f32x4{0.f, 0.f, 0.f, 0.f};
        }
        if constexpr (EPI == EPI_QKV) {
            u16* C = (u16*)(mat == 0 ? g.C[0] : (mat == 1 ? g.C[1] : g.C[2]));
            asm volatile("s_waitcnt lgkmcnt(0)" ::: "memory");
#pragma unroll
            for (int it = 0; it < TN / 32; ++it) {
                const int idx = it * 64 + lane, rr = idx / (TN / 8), cc = idx % (TN / 8);
                u32x4 w = *(const u32x4*)(ep + rr * 128 + ((cc ^ (rr & 7)) << 4));
                const int mm = m0 + wm * TM + 16 * mi + rr, nn = n0 + wn * TN + cc * 8;
                *(u32x4*)(C + ((size_t)(nn >> 7) * SEQ + mm) * HD + (nn & 127)) = w;
            }
            asm volatile("s_waitcnt lgkmcnt(0)" ::: "memory");
        }
    }
    }
}

constexpr float SCALE = 0.08838834764831845f;
constexpr int ATT_LDS = 4 * 32768 + 8 * 64 * 4;
__device__ __forceinline__ int crow(int r, int hi) { return (r & 3) + 8 * (r >> 2) + 4 * hi; }
__device__ __forceinline__ int v_rd_base(int lane) { return ((lane & 3) << 3) | (((lane >> 2) & 3) << 6) | (((lane >> 4) & 1) << 5) | (((lane >> 5) & 1) << 8); }

__global__ __launch_bounds__(512, 1) void attn_kernel(const u16* __restrict__ Q, const u16* __restrict__ K, const u16* __restrict__ V,
                                                      u16* __restrict__ ctx) {
    extern __shared__ __attribute__((aligned(16))) char lds[];
    const int tid = threadIdx.x, wid = __builtin_amdgcn_readfirstlane(tid >> 6);
    const int rg = wid & 3, par = wid >> 2;
    const int h = (blockIdx.x & 7) + 8 * (blockIdx.x >> 6), xq = (blockIdx.x >> 3) & 7;
    const u16* Kh = K + (size_t)h * SEQ * HD; const u16* Vh = V + (size_t)h * SEQ * HD;
    char* K_lds = lds; char* V_lds = lds + 65536;
    float* al_l = (float*)(lds + 4 * 32768) + wid * 64;

    unsigned koff, voff, loff;
    auto stageK = [&](int s, int buf) {
        const char* kb_ = (const char*)Kh + (size_t)s * 32768;
#pragma unroll
        for (int i = 0; i < 4; ++i)
            __builtin_amdgcn_global_load_lds((const unsigned*)(kb_ + koff + i * 8192), (unsigned*)(K_lds + buf * 32768 + i * 8192 + loff), 16, 0, 0);
    };
    auto stageV = [&](int s, int buf) {
        const char* vb_ = (const char*)Vh + (size_t)s * 32768;
#pragma unroll
        for (int i = 0; i < 4; ++i)
            __builtin_amdgcn_global_load_lds((const unsigned*)(vb_ + voff + i * 8192), (unsigned*)(V_lds + buf * 32768 + i * 8192 + loff), 16, 0, 0);
    };
    constexpr float C2 = 1.4426950408889634f * SCALE;
#define QKT(P0, P1, buf, act) do { { \
        P0 = f32x16{}; P1 = f32x16{}; const char* kb[4]; \
        _Pragma("unroll") for (int dd = 0; dd < 4; ++dd) kb[dd] = K_lds + (buf) * 32768 + (par * 64 + r32) * 256 + (((dd * 16 + hi * 8) * 2) ^ ((r32 & 7) << 4)); \
        _Pragma("unroll") for (int d0 = 0; d0 < 8; ++d0) { const char* a_ = kb[d0 & 3] + (d0 >> 2) * 128; \
            bf16x8 b0 = *(const bf16x8*)a_, b1 = *(const bf16x8*)(a_ + 32 * 256); \
            P0 = __builtin_amdgcn_mfma_f32_32x32x16_bf16(b0, qr[d0], P0, 0, 0, 0); \
            P1 = __builtin_amdgcn_mfma_f32_32x32x16_bf16(b1, qr[d0], P1, 0, 0, 0); } } } while (0)
#define PARTSM(P0, P1, s_, alpha) do { \
        const int kbase_ = 128 * (s_) + 64 * par; \
        if (kbase_ + 63 > qrow0) { const int dq = qrow0 + r32 - kbase_ - 4 * hi; const float NEG_ = -__builtin_inff(); \
            _Pragma("unroll") for (int r = 0; r < 16; ++r) { const int c = (r & 3) + 8 * (r >> 2); \
                if (dq - c < 0) P0[r] = NEG_; if (dq - c - 32 < 0) P1[r] = NEG_; } } \
        float pmax = P0[0]; \
        _Pragma("unroll") for (int r = 1; r < 16; ++r) pmax = fmaxf(pmax, P0[r]); \
        _Pragma("unroll") for (int r = 0; r < 16; ++r) pmax = fmaxf(pmax, P1[r]); \
        { auto rr = __builtin_amdgcn_permlane32_swap(__float_as_uint(pmax), __float_as_uint(pmax), false, false); \
          pmax = fmaxf(__uint_as_float(rr[0]), __uint_as_float(rr[1])); } \
          \
        if (__builtin_expect(__all((pmax - m_reg) * SCALE <= 8.f), 1)) { alpha = 1.f; } \
        else { const float mn = fmaxf(m_reg, pmax); alpha = __builtin_amdgcn_exp2f((m_reg - mn) * C2); m_reg = mn; } \
        const float mnL = -m_reg * C2; \
        _Pragma("unroll") for (int r = 0; r < 16; ++r) { P0[r] = __builtin_amdgcn_exp2f(fmaf(P0[r], C2, mnL)); P1[r] = fmaf(P1[r], C2, mnL); } } while (0)
#define PK4(P, B_, OUT) do { unsigned a0 = cvtpk(P[B_+0], P[B_+1]), a1 = cvtpk(P[B_+2], P[B_+3]);                          \
        unsigned b0 = cvtpk(P[B_+4], P[B_+5]), b1 = cvtpk(P[B_+6], P[B_+7]);                                             \
        auto r0 = __builtin_amdgcn_permlane32_swap(a0, b0, false, false); auto r1 = __builtin_amdgcn_permlane32_swap(a1, b1, false, false); \
        u32x4 w = {r0[0], r1[0], r0[1], r1[1]}; OUT = *reinterpret_cast<bf16x8*>(&w); } while (0)
#define FINSM(P0, P1, alpha) do { float ps = 0.f; \
        _Pragma("unroll") for (int r = 0; r < 16; ++r) { P1[r] = __builtin_amdgcn_exp2f(P1[r]); ps += P0[r]; } \
        _Pragma("unroll") for (int r = 0; r < 16; ++r) ps += P1[r]; \
        { auto rr = __builtin_amdgcn_permlane32_swap(__float_as_uint(ps), __float_as_uint(ps), false, false); \
          ps = __uint_as_float(rr[0]) + __uint_as_float(rr[1]); } \
        l_reg = l_reg * (alpha) + ps; \
        PK4(P0, 0, pa0); PK4(P0, 8, pa1); PK4(P1, 0, pa2); PK4(P1, 8, pa3); } while (0)
#define RESC(a) do { if (__any((a) < 1.f)) { if (hi == 0) al_l[r32] = (a); asm volatile("s_waitcnt lgkmcnt(0)" ::: "memory"); \
        _Pragma("unroll") for (int d_ = 0; d_ < 4; ++d_) _Pragma("unroll") for (int r = 0; r < 16; ++r) o[d_][r] *= al_l[crow(r, hi)]; } } while (0)
#define TRRD(dst, off) asm volatile("ds_read_b64_tr_b16 %0, %1 offset:%2" : "=&v"(dst) : "v"(vb), "i"(off) : "memory")
#define PV_D0(d0) do { s16x4 l0, l1, l2, l3, h0, h1, h2, h3; constexpr int b_ = (d0) * 512; \
        TRRD(l0, b_); TRRD(h0, b_ + 2048); TRRD(l1, b_ + 4096); TRRD(h1, b_ + 6144); TRRD(l2, b_ + 8192); TRRD(h2, b_ + 10240); TRRD(l3, b_ + 12288); TRRD(h3, b_ + 14336); \
        asm volatile("s_waitcnt lgkmcnt(0)" ::: "memory"); SBAR(); \
        o[d0] = __builtin_amdgcn_mfma_f32_32x32x16_bf16(pa0, (bf16x8){l0[0], l0[1], l0[2], l0[3], h0[0], h0[1], h0[2], h0[3]}, o[d0], 0, 0, 0);   \
        o[d0] = __builtin_amdgcn_mfma_f32_32x32x16_bf16(pa1, (bf16x8){l1[0], l1[1], l1[2], l1[3], h1[0], h1[1], h1[2], h1[3]}, o[d0], 0, 0, 0);   \
        o[d0] = __builtin_amdgcn_mfma_f32_32x32x16_bf16(pa2, (bf16x8){l2[0], l2[1], l2[2], l2[3], h2[0], h2[1], h2[2], h2[3]}, o[d0], 0, 0, 0);   \
        o[d0] = __builtin_amdgcn_mfma_f32_32x32x16_bf16(pa3, (bf16x8){l3[0], l3[1], l3[2], l3[3], h3[0], h3[1], h3[2], h3[3]}, o[d0], 0, 0, 0); } while (0)
#define PV(buf) do { const int vb = vb0 + (buf) * 32768; PV_D0(0); PV_D0(1); PV_D0(2); PV_D0(3); } while (0)
#define ACT(s_) (128 * (s_) + 64 * par <= qrow0 + 31)
#define ITER(PX0, PX1, alX, PY0, PY1, alY, s_, E) do { \
        asm volatile("s_waitcnt vmcnt(0)" ::: "memory"); __syncthreads(); \
        if ((s_) + 2 < nsteps) stageK((s_) + 2, E); \
        stageV((s_) + 1, 1 - (E)); SBAR(); \
        QKT(PX0, PX1, 1 - (E), ACT((s_) + 1)); \
        FINSM(PY0, PY1, alY); SBAR(); \
        PV(E); PARTSM(PX0, PX1, (s_) + 1, alX); \
        RESC(alX); } while (0)

    for (int pass = 0; pass < 2; ++pass) {
        const int qb = pass ? 15 - xq : xq, nsteps = qb + 1;
        const int qrow0 = qb * 128 + rg * 32;
        int tz = tid; asm volatile("" : "+v"(tz));
        int r32 = tz & 31, hi = (tz >> 5) & 1;
        const int vb0 = (int)(uintptr_t)V_lds + par * 16384 + v_rd_base(tz & 63);
        koff = (unsigned)((tz >> 4) * 256 + (((tz & 15) ^ ((tz >> 4) & 7)) << 4)); loff = (unsigned)tz * 16;
        { const int sub = tz >> 5, r7 = (tz >> 2) & 7, c8 = tz & 3, kk = (sub >> 2) * 8 + r7;
          const int k = (kk & ~0xC) | ((kk & 4) << 1) | ((kk & 8) >> 1);
          voff = (unsigned)(k * 256 + ((sub & 3) * 32 + c8 * 8) * 2); }
        bf16x8 qr[8];
#pragma unroll
        for (int d0 = 0; d0 < 8; ++d0) qr[d0] = *(const bf16x8*)(Q + ((size_t)h * SEQ + qrow0 + r32) * HD + d0 * 16 + hi * 8);
        float m_reg = -1e30f, l_reg = 0.f; f32x16 o[4] = {};
        f32x16 pA0, pA1, pB0, pB1; float alA, alB; bf16x8 pa0, pa1, pa2, pa3;
        __syncthreads();
        stageK(0, 0); stageV(0, 0);
        asm volatile("s_waitcnt vmcnt(0)" ::: "memory"); __syncthreads();
        if (nsteps > 1) stageK(1, 1);
        QKT(pA0, pA1, 0, ACT(0)); PARTSM(pA0, pA1, 0, alA);
        int s = 0;
        for (; s + 2 < nsteps; s += 2) {
            ITER(pB0, pB1, alB, pA0, pA1, alA, s, 0);
            ITER(pA0, pA1, alA, pB0, pB1, alB, s + 1, 1);
        }
        if (s + 1 < nsteps) {
            ITER(pB0, pB1, alB, pA0, pA1, alA, s, 0);
            asm volatile("s_waitcnt vmcnt(0)" ::: "memory"); __syncthreads();
            FINSM(pB0, pB1, alB); SBAR(); PV(1);
        } else {
            asm volatile("s_waitcnt vmcnt(0)" ::: "memory"); __syncthreads();
            FINSM(pA0, pA1, alA); SBAR(); PV(0);
        }
        __syncthreads();
        tz = tid; asm volatile("" : "+v"(tz)); r32 = tz & 31; hi = (tz >> 5) & 1;
        float* cO = (float*)lds + rg * (32 * 128 + 64);
        if (par == 1) {
#pragma unroll
            for (int d = 0; d < 4; ++d)
#pragma unroll
                for (int r = 0; r < 16; ++r) cO[crow(r, hi) * 128 + d * 32 + r32] = o[d][r];
            if (hi == 0) { cO[4096 + r32] = m_reg; cO[4096 + 32 + r32] = l_reg; }
        }
        __syncthreads();
        if (par == 0) {
            const float m1 = cO[4096 + r32], l1 = cO[4096 + 32 + r32];
            const float mm = fmaxf(m_reg, m1);
            const float a0 = __builtin_amdgcn_exp2f((m_reg - mm) * C2), a1 = __builtin_amdgcn_exp2f((m1 - mm) * C2);
            const float linv = 1.f / (l_reg * a0 + l1 * a1);
            if (hi == 0) { al_l[r32] = a0 * linv; al_l[32 + r32] = a1 * linv; }
            asm volatile("s_waitcnt lgkmcnt(0)" ::: "memory");
#pragma unroll
            for (int r = 0; r < 16; ++r) {
                const int orow = crow(r, hi); const float c0 = al_l[orow], c1 = al_l[32 + orow];
#pragma unroll
                for (int d = 0; d < 4; ++d) {
                    const float v = o[d][r] * c0 + cO[orow * 128 + d * 32 + r32] * c1;
                    const float vn = __shfl_xor(v, 1);
                    if ((r32 & 1) == 0) *(unsigned*)(ctx + (size_t)(qrow0 + orow) * (NH * HD) + h * HD + d * 32 + r32) = cvtpk(v, vn);
                }
            }
        }
    }
}

extern "C" void kernel_launch(void* const* d_in, const int* in_sizes, int n_in, void* d_out, int out_size, void* d_ws, size_t ws_size,
                              hipStream_t stream) {
    const u16* x = (const u16*)d_in[0]; const u16* ckv = (const u16*)d_in[1];
    const u16* wq = (const u16*)d_in[2]; const u16* wuk = (const u16*)d_in[3]; const u16* wuv = (const u16*)d_in[4]; const u16* wo = (const u16*)d_in[5];
    const u16* w1 = (const u16*)d_in[7]; const u16* b1 = (const u16*)d_in[8]; const u16* w2 = (const u16*)d_in[9]; const u16* b2 = (const u16*)d_in[10];
    u16* out = (u16*)d_out;
    char* ws = (char*)d_ws;
    u16* xn = (u16*)ws;                                   ws += (size_t)SEQ * DM * 2;
    u16* qb = (u16*)ws;                                   ws += (size_t)SEQ * NH * HD * 2;
    u16* kb = (u16*)ws;                                   ws += (size_t)SEQ * NH * HD * 2;
    u16* vb = (u16*)ws;                                   ws += (size_t)SEQ * NH * HD * 2;
    u16* ctx = (u16*)ws;                                  ws += (size_t)SEQ * NH * HD * 2;
    u16* hbuf = (u16*)ws;                                 ws += (size_t)SEQ * DM * 2;
    u16* hn = (u16*)ws;                                   ws += (size_t)SEQ * DM * 2;
    u16* fbuf = (u16*)ws;                                 ws += (size_t)SEQ * DFFP * 2;
    float* part = (float*)ws;                             ws += (size_t)4 * SEQ * DM * 4;

    constexpr int KS = 4;
    auto gq = gemm_kernel<128, 256, 2, 4, EPI_QKV, 3>;
    auto go = gemm_kernel<64, 128, 2, 2, EPI_PART, 3>;
    auto g1 = gemm_kernel<64, 128, 2, 2, EPI_GELU, 3>;
    auto g2 = gemm_kernel<64, 64, 2, 2, EPI_FFN2, 3>;
    (void)hipFuncSetAttribute((const void*)gq, hipFuncAttributeMaxDynamicSharedMemorySize, 3 * (128 + 256) * 128 + 8 * 16 * 128);
    (void)hipFuncSetAttribute((const void*)go, hipFuncAttributeMaxDynamicSharedMemorySize, 3 * (64 + 128) * 128);
    (void)hipFuncSetAttribute((const void*)g1, hipFuncAttributeMaxDynamicSharedMemorySize, 3 * (64 + 128) * 128);
    (void)hipFuncSetAttribute((const void*)g2, hipFuncAttributeMaxDynamicSharedMemorySize, 3 * (64 + 64) * 128);
    (void)hipFuncSetAttribute((const void*)attn_kernel, hipFuncAttributeMaxDynamicSharedMemorySize, ATT_LDS);

    ln1_kernel<<<SEQ / 4, 256, 0, stream>>>(x, xn);
    {
        GemmArgs g = {};
        g.A[0] = xn; g.A[1] = ckv; g.A[2] = ckv; g.B[0] = wq; g.B[1] = wuk; g.B[2] = wuv; g.C[0] = qb; g.C[1] = kb; g.C[2] = vb;
        g.lda = DM; g.ldb = NH * HD; g.M = SEQ; g.N = NH * HD; g.K = DM; g.Kv = DM; g.ksplit = 1;
        g.nTn = (NH * HD) / 256;
        g.ksplit = 3; gq<<<256, 512, 3 * (128 + 256) * 128 + 8 * 16 * 128, stream>>>(g);
    }
    attn_kernel<<<NH * 8, 512, ATT_LDS, stream>>>(qb, kb, vb, ctx);
    {
        GemmArgs g = {};
        g.A[0] = ctx; g.B[0] = wo; g.C[0] = part; g.lda = NH * HD; g.ldb = DM; g.M = SEQ; g.N = DM; g.K = NH * HD / KS; g.Kv = NH * HD;
        g.nTn = DM / 128; g.ksplit = KS;
        go<<<512, 256, 3 * (64 + 128) * 128, stream>>>(g);
    }
    oreduce_ln2_kernel<KS><<<SEQ / 4, 256, 0, stream>>>(part, x, hbuf, hn);
    {
        GemmArgs g = {};
        g.A[0] = hn; g.B[0] = w1; g.C[0] = fbuf; g.bias = b1; g.lda = DM; g.ldb = DFF; g.M = SEQ; g.N = DFF; g.K = DM; g.Kv = DM;
        g.nTn = (DFF + 127) / 128; g.ksplit = 1;
        g1<<<352, 256, 3 * (64 + 128) * 128, stream>>>(g);
    }
    {
        GemmArgs g = {};
        g.A[0] = fbuf; g.B[0] = w2; g.C[0] = out; g.bias = b2; g.res = hbuf; g.lda = DFFP; g.ldb = DM; g.M = SEQ; g.N = DM; g.K = DFFP; g.Kv = DFF;
        g.nTn = DM / 64; g.ksplit = 1;
        g2<<<256, 256, 3 * (64 + 64) * 128, stream>>>(g);
    }
}
```

```cpp
#include <hip/hip_runtime.h>
#include <hip/hip_bf16.h>
#include <stdint.h>

typedef short bf16x8 __attribute__((ext_vector_type(8)));
typedef short s16x4 __attribute__((ext_vector_type(4)));
typedef float f32x4 __attribute__((ext_vector_type(4)));
typedef float f32x16 __attribute__((ext_vector_type(16)));
typedef unsigned u32x4 __attribute__((ext_vector_type(4)));
typedef unsigned u32x2 __attribute__((ext_vector_type(2)));
typedef unsigned short u16;

#define SBAR() __builtin_amdgcn_sched_barrier(0)

constexpr int SEQ = 2048, DM = 512, NH = 32, HD = 128, DC = 512, DFF = 1376, DFFP = 1408;

__device__ __forceinline__ unsigned cvtpk(float lo, float hi) {
    typedef float f32x2_ __attribute__((ext_vector_type(2))); typedef __bf16 bf16x2_ __attribute__((ext_vector_type(2)));
    f32x2_ v = {lo, hi}; bf16x2_ b = __builtin_convertvector(v, bf16x2_);
    return __builtin_bit_cast(unsigned, b);
}
__device__ __forceinline__ float bf2f(u16 v) { return __uint_as_float(((unsigned)v) << 16); }
__device__ __forceinline__ float bfr(float v) { return __uint_as_float(cvtpk(v, v) << 16); }

__device__ __forceinline__ float wave_sum(float v) {
#pragma unroll
    for (int o = 32; o > 0; o >>= 1) v += __shfl_xor(v, o);
    return v;
}
__device__ __forceinline__ void ln8(float* x) {
    float s = 0;
#pragma unroll
    for (int i = 0; i < 8; ++i) s += x[i];
    s = wave_sum(s);
    const float mean = s * (1.f / 512.f);
    float q = 0;
#pragma unroll
    for (int i = 0; i < 8; ++i) { const float d = x[i] - mean; q += d * d; }
    q = wave_sum(q);
    const float mb = bfr(mean);
    const float vb = bfr(q * (1.f / 512.f));
    const float den = bfr(sqrtf(bfr(vb + 1e-5f)));
#pragma unroll
    for (int i = 0; i < 8; ++i) x[i] = bfr(bfr(x[i] - mb) / den);
}
__device__ __forceinline__ void unpack8(u32x4 w, float* x) {
#pragma unroll
    for (int i = 0; i < 4; ++i) { x[2 * i] = __uint_as_float(w[i] << 16); x[2 * i + 1] = __uint_as_float(w[i] & 0xffff0000u); }
}
__device__ __forceinline__ u32x4 pack8f(const float* x) {
    u32x4 w = {cvtpk(x[0], x[1]), cvtpk(x[2], x[3]), cvtpk(x[4], x[5]), cvtpk(x[6], x[7])};
    return w;
}

__global__ __launch_bounds__(256) void ln1_kernel(const u16* __restrict__ x, u16* __restrict__ xn) {
    const int row = blockIdx.x * 4 + (threadIdx.x >> 6), lane = threadIdx.x & 63;
    u32x4 w = *(const u32x4*)(x + (size_t)row * DM + lane * 8);
    float v[8]; unpack8(w, v); ln8(v);
    *(u32x4*)(xn + (size_t)row * DM + lane * 8) = pack8f(v);
}

template <int KS>
__global__ __launch_bounds__(256) void oreduce_ln2_kernel(const float* __restrict__ part, const u16* __restrict__ x,
                                                          u16* __restrict__ h, u16* __restrict__ hn) {
    const int row = blockIdx.x * 4 + (threadIdx.x >> 6), lane = threadIdx.x & 63;
    float a[8];
#pragma unroll
    for (int i = 0; i < 8; ++i) a[i] = 0.f;
#pragma unroll
    for (int s = 0; s < KS; ++s) {
        const float* p = part + ((size_t)s * SEQ + row) * DM + lane * 8;
        f32x4 p0 = *(const f32x4*)p, p1 = *(const f32x4*)(p + 4);
#pragma unroll
        for (int i = 0; i < 4; ++i) { a[i] += p0[i]; a[4 + i] += p1[i]; }
    }
    u32x4 w = *(const u32x4*)(x + (size_t)row * DM + lane * 8);
    float v[8]; unpack8(w, v);
#pragma unroll
    for (int i = 0; i < 8; ++i) v[i] = bfr(v[i] + bfr(a[i]));
    *(u32x4*)(h + (size_t)row * DM + lane * 8) = pack8f(v);
    ln8(v);
    *(u32x4*)(hn + (size_t)row * DM + lane * 8) = pack8f(v);
}

enum { EPI_QKV = 0, EPI_PART = 1, EPI_GELU = 2, EPI_FFN2 = 3 };
struct GemmArgs {
    const u16* A[3]; const u16* B[3]; void* C[3];
    const u16* bias; const u16* res;
    int lda, ldb, M, N, K, Kv, nTn, ksplit;
};

__device__ __forceinline__ int a_lds_byte(int r, int c) {
    const int st = (r >> 4) * 2 + (c >> 5), ob = (r & 15) * 64 + (c & 31) * 2;
    return st * 1024 + (ob ^ (((ob >> 9) & 1) << 5));
}
__device__ __forceinline__ void a_stage_rc(int b, int& R, int& C) {
    const int st = b >> 10, sb = b & 1023, swz = sb ^ (((sb >> 9) & 1) << 5);
    R = (st >> 1) * 16 + (swz >> 6); C = (st & 1) * 32 + ((swz & 63) >> 1);
}

template <int BM, int BN, int WM, int WN, int EPI, int NST>
__global__ __launch_bounds__(WM * WN * 64) void gemm_kernel(GemmArgs g) {
    constexpr int NT = WM * WN * 64, TM = BM / WM, TN = BN / WN, MI = TM / 16, NI = TN / 16;
    constexpr int A_BYTES = BM * 64 * 2, B_BYTES = BN * 64 * 2, NSUB = BN / 32;
    extern __shared__ __attribute__((aligned(16))) char smem[];
    const int tid = threadIdx.x, wid = tid >> 6, lane = tid & 63, wm = wid / WN, wn = wid % WN;
    const int nkt = g.K / 64, tilesPerZ = g.nTn * (g.M / BM), ntiles = tilesPerZ * g.ksplit;
    int ld_t = blockIdx.x, ld_kt = 0;
    constexpr int NLA = A_BYTES / (NT * 16), NLB = B_BYTES / (NT * 16);
    constexpr bool CLAMP = (EPI == EPI_GELU || EPI == EPI_FFN2);
    unsigned aoff[NLA], boff[NLB]; int brow[NLB], bcol[NLB];
#pragma unroll
    for (int i = 0; i < NLA; ++i) { int R, C; a_stage_rc((i * NT + tid) * 16, R, C); aoff[i] = (unsigned)(R * g.lda + C) * 2u; }
#pragma unroll
    for (int i = 0; i < NLB; ++i) {
        const int P = i * NT + tid, s = P >> 5, r7 = (P >> 2) & 7, cx = P & 3;
        const int row = 8 * (s / NSUB) + r7, ch = 4 * (s % NSUB) + (cx ^ ((row >> 2) & 3));
        brow[i] = row; bcol[i] = ch * 8; boff[i] = (unsigned)(row * g.ldb + ch * 8) * 2u;
    }
    const char* ldA = nullptr; const char* ldB = nullptr; int ld_n0 = 0, ld_k0 = 0;
    auto decode_ld = [&]() {
        if (ld_t >= ntiles) return;
        const int z = ld_t / tilesPerZ, rem = ld_t - z * tilesPerZ, tn = rem % g.nTn, tm = rem / g.nTn;
        const int mat = (EPI == EPI_QKV) ? z : 0, ksp = (EPI == EPI_PART) ? z : 0;
        const u16* A = mat == 0 ? g.A[0] : (mat == 1 ? g.A[1] : g.A[2]); const u16* B = mat == 0 ? g.B[0] : (mat == 1 ? g.B[1] : g.B[2]);
        ld_n0 = tn * BN; ld_k0 = ksp * g.K;
        ldA = (const char*)(A + (size_t)(tm * BM) * g.lda + ld_k0); ldB = (const char*)(B + (size_t)ld_k0 * g.ldb + ld_n0);
    };
    decode_ld();
    auto stage = [&](int buf) {
        if (ld_t >= ntiles) return;
        char* sa = smem + buf * (A_BYTES + B_BYTES) + tid * 16; char* sb = sa + A_BYTES;
#pragma unroll
        for (int i = 0; i < NLA; ++i)
            __builtin_amdgcn_global_load_lds((const unsigned*)(ldA + aoff[i]), (unsigned*)(sa + i * NT * 16), 16, 0, 0);
#pragma unroll
        for (int i = 0; i < NLB; ++i) {
            if constexpr (CLAMP) {
                int kr = ld_k0 + brow[i]; if (kr > g.Kv - 1) kr = g.Kv - 1;
                int nc = ld_n0 + bcol[i]; if (nc > g.N - 8) nc = g.N - 8;
                const u16* B = g.B[0];
                __builtin_amdgcn_global_load_lds((const unsigned*)(B + (size_t)kr * g.ldb + nc), (unsigned*)(sb + i * NT * 16), 16, 0, 0);
            } else
                __builtin_amdgcn_global_load_lds((const unsigned*)(ldB + boff[i]), (unsigned*)(sb + i * NT * 16), 16, 0, 0);
        }
        ldA += 128; ldB += (size_t)g.ldb * 128; ld_k0 += 64;
        if (++ld_kt == nkt) { ld_kt = 0; ld_t += gridDim.x; decode_ld(); }
    };

    f32x4 acc[MI][NI];
#pragma unroll
    for (int i = 0; i < MI; ++i)
#pragma unroll
        for (int j = 0; j < NI; ++j) acc[i][j] = f32x4{0.f, 0.f, 0.f, 0.f};

    const int g4 = lane >> 4, i16 = lane & 15, q4 = i16 >> 2, p4 = i16 & 3;
    const int a_base = a_lds_byte(i16, 8 * g4);
    int b_base[2][2];
#pragma unroll
    for (int t = 0; t < 2; ++t)
#pragma unroll
        for (int par = 0; par < 2; ++par) {
            const int row = 8 * g4 + 4 * t + q4, chl = 2 * par + (p4 >> 1);
            b_base[t][par] = 512 * (g4 * NSUB + (wn * TN >> 5)) + 64 * (4 * t + q4) + 16 * (chl ^ ((row >> 2) & 3)) + 8 * (p4 & 1);
        }

    constexpr int LPS = (A_BYTES + B_BYTES) / (NT * 16);
#pragma unroll
    for (int i = 0; i < NST - 1; ++i) stage(i);
    int cur = 0;
    for (int t = blockIdx.x; t < ntiles; t += gridDim.x) {
    const int z = t / tilesPerZ, rem_ = t - z * tilesPerZ, tn = rem_ % g.nTn, tm = rem_ / g.nTn;
    const int mat = (EPI == EPI_QKV) ? z : 0, ksp = (EPI == EPI_PART) ? z : 0;
    const int m0 = tm * BM, n0 = tn * BN;
    for (int kt = 0; kt < nkt; ++kt) {
        if (ld_t < ntiles) asm volatile("s_waitcnt vmcnt(%0)" :: "i"(LPS * (NST - 2)) : "memory");
        else asm volatile("s_waitcnt vmcnt(0)" ::: "memory");
        __builtin_amdgcn_s_barrier();
        stage(cur >= 1 ? cur - 1 : NST - 1);
        const char* sa = smem + cur * (A_BYTES + B_BYTES);
        cur = cur == NST - 1 ? 0 : cur + 1;
        const unsigned sbo = (unsigned)(uintptr_t)(sa + A_BYTES);
#pragma unroll
        for (int ks = 0; ks < 2; ++ks) {
            bf16x8 af[MI], bfr_[NI];
#pragma unroll
            for (int mi = 0; mi < MI; ++mi) af[mi] = *(const bf16x8*)(sa + a_base + ((wm * MI + mi) * 2 + ks) * 1024);
#pragma unroll
            for (int ni = 0; ni < NI; ++ni) {
                const int off = 512 * (4 * ks * NSUB + (ni >> 1));
                s16x4 lo, hi;
                const unsigned a0 = sbo + b_base[0][ni & 1], a1 = sbo + b_base[1][ni & 1];
                asm volatile("ds_read_b64_tr_b16 %0, %1 offset:%2" : "=&v"(lo) : "v"(a0), "i"(off) : "memory");
                asm volatile("ds_read_b64_tr_b16 %0, %1 offset:%2" : "=&v"(hi) : "v"(a1), "i"(off) : "memory");
                bfr_[ni] = (bf16x8){lo[0], lo[1], lo[2], lo[3], hi[0], hi[1], hi[2], hi[3]};
            }
            asm volatile("s_waitcnt lgkmcnt(0)" ::: "memory"); SBAR();
#pragma unroll
            for (int mi = 0; mi < MI; ++mi)
#pragma unroll
                for (int ni = 0; ni < NI; ++ni)
                    acc[mi][ni] = __builtin_amdgcn_mfma_f32_16x16x32_bf16(bfr_[ni], af[mi], acc[mi][ni], 0, 0, 0);
        }
    }
#pragma unroll
    for (int mi = 0; mi < MI; ++mi) {
        const int m = m0 + wm * TM + 16 * mi + i16;
        char* ep = smem + NST * (A_BYTES + B_BYTES) + wid * (16 * 128);
#pragma unroll
        for (int ni = 0; ni < NI; ++ni) {
            const int n = n0 + wn * TN + 16 * ni + 4 * g4;
            f32x4 v = acc[mi][ni];
            if constexpr (EPI == EPI_QKV) {
                u32x2 w = {cvtpk(v[0], v[1]), cvtpk(v[2], v[3])};
                *(u32x2*)(ep + i16 * 128 + (((2 * ni + (g4 >> 1)) ^ (i16 & 7)) << 4) + (g4 & 1) * 8) = w;
            } else if constexpr (EPI == EPI_PART) {
                *(f32x4*)((float*)g.C[0] + ((size_t)ksp * g.M + m) * g.N + n) = v;
            } else if constexpr (EPI == EPI_GELU) {
                float o[4];
                if (n < g.N) {
                    u32x2 bw = *(const u32x2*)(g.bias + n);
                    const float b[4] = {__uint_as_float(bw[0] << 16), __uint_as_float(bw[0] & 0xffff0000u),
                                        __uint_as_float(bw[1] << 16), __uint_as_float(bw[1] & 0xffff0000u)};
#pragma unroll
                    for (int j = 0; j < 4; ++j) {
                        const float xx = bfr(bfr(v[j]) + b[j]);
                        const float u = 0.7978845608028654f * (xx + 0.044715f * xx * xx * xx);
                        const float th = 1.f - 2.f / (__expf(2.f * u) + 1.f);
                        o[j] = 0.5f * xx * (1.f + th);
                    }
                } else { o[0] = o[1] = o[2] = o[3] = 0.f; }
                if (n < DFFP) {
                    u32x2 w = {cvtpk(o[0], o[1]), cvtpk(o[2], o[3])};
                    *(u32x2*)((u16*)g.C[0] + (size_t)m * DFFP + n) = w;
                }
            } else {
                u32x2 bw = *(const u32x2*)(g.bias + n);
                u32x2 rw = *(const u32x2*)(g.res + (size_t)m * DM + n);
                const float b[4] = {__uint_as_float(bw[0] << 16), __uint_as_float(bw[0] & 0xffff0000u),
                                    __uint_as_float(bw[1] << 16), __uint_as_float(bw[1] & 0xffff0000u)};
                const float r[4] = {__uint_as_float(rw[0] << 16), __uint_as_float(rw[0] & 0xffff0000u),
                                    __uint_as_float(rw[1] << 16), __uint_as_float(rw[1] & 0xffff0000u)};
                float o[4];
#pragma unroll
                for (int j = 0; j < 4; ++j) o[j] = r[j] + bfr(bfr(v[j]) + b[j]);
                u32x2 w = {cvtpk(o[0], o[1]), cvtpk(o[2], o[3])};
                *(u32x2*)((u16*)g.C[0] + (size_t)m * DM + n) = w;
            }
            acc[mi][ni] = f32x4{0.f, 0.f, 0.f, 0.f};
        }
        if constexpr (EPI == EPI_QKV) {
            u16* C = (u16*)(mat == 0 ? g.C[0] : (mat == 1 ? g.C[1] : g.C[2]));
            asm volatile("s_waitcnt lgkmcnt(0)" ::: "memory");
#pragma unroll
            for (int it = 0; it < TN / 32; ++it) {
                const int idx = it * 64 + lane, rr = idx / (TN / 8), cc = idx % (TN / 8);
                u32x4 w = *(const u32x4*)(ep + rr * 128 + ((cc ^ (rr & 7)) << 4));
                const int mm = m0 + wm * TM + 16 * mi + rr, nn = n0 + wn * TN + cc * 8;
                *(u32x4*)(C + ((size_t)(nn >> 7) * SEQ + mm) * HD + (nn & 127)) = w;
            }
            asm volatile("s_waitcnt lgkmcnt(0)" ::: "memory");
        }
    }
    }
}

constexpr float SCALE = 0.08838834764831845f;
constexpr int ATT_LDS = 4 * 32768 + 8 * 64 * 4;
__device__ __forceinline__ int crow(int r, int hi) { return (r & 3) + 8 * (r >> 2) + 4 * hi; }
__device__ __forceinline__ int v_rd_base(int lane) { return ((lane & 3) << 3) | (((lane >> 2) & 3) << 6) | (((lane >> 4) & 1) << 5) | (((lane >> 5) & 1) << 8); }

__global__ __launch_bounds__(512, 1) void attn_kernel(const u16* __restrict__ Q, const u16* __restrict__ K, const u16* __restrict__ V,
                                                      u16* __restrict__ ctx) {
    extern __shared__ __attribute__((aligned(16))) char lds[];
    const int tid = threadIdx.x, wid = __builtin_amdgcn_readfirstlane(tid >> 6);
    const int rg = wid & 3, par = wid >> 2;
    const int h = (blockIdx.x & 7) + 8 * (blockIdx.x >> 6), xq = (blockIdx.x >> 3) & 7;
    const u16* Kh = K + (size_t)h * SEQ * HD; const u16* Vh = V + (size_t)h * SEQ * HD;
    char* K_lds = lds; char* V_lds = lds + 65536;
    float* al_l = (float*)(lds + 4 * 32768) + wid * 64;

    unsigned koff, voff, loff;
    auto stageK = [&](int s, int buf) {
        const char* kb_ = (const char*)Kh + (size_t)s * 32768;
#pragma unroll
        for (int i = 0; i < 4; ++i)
            __builtin_amdgcn_global_load_lds((const unsigned*)(kb_ + koff + i * 8192), (unsigned*)(K_lds + buf * 32768 + i * 8192 + loff), 16, 0, 0);
    };
    auto stageV = [&](int s, int buf) {
        const char* vb_ = (const char*)Vh + (size_t)s * 32768;
#pragma unroll
        for (int i = 0; i < 4; ++i)
            __builtin_amdgcn_global_load_lds((const unsigned*)(vb_ + voff + i * 8192), (unsigned*)(V_lds + buf * 32768 + i * 8192 + loff), 16, 0, 0);
    };
    constexpr float C2 = 1.4426950408889634f * SCALE;
#define QKT(P0, P1, buf, act) do { { \
        P0 = f32x16{}; P1 = f32x16{}; const char* kb[4]; \
        _Pragma("unroll") for (int dd = 0; dd < 4; ++dd) kb[dd] = K_lds + (buf) * 32768 + (par * 64 + r32) * 256 + (((dd * 16 + hi * 8) * 2) ^ ((r32 & 7) << 4)); \
        _Pragma("unroll") for (int d0 = 0; d0 < 8; ++d0) { const char* a_ = kb[d0 & 3] + (d0 >> 2) * 128; \
            bf16x8 b0 = *(const bf16x8*)a_, b1 = *(const bf16x8*)(a_ + 32 * 256); \
            P0 = __builtin_amdgcn_mfma_f32_32x32x16_bf16(b0, qr[d0], P0, 0, 0, 0); \
            P1 = __builtin_amdgcn_mfma_f32_32x32x16_bf16(b1, qr[d0], P1, 0, 0, 0); } } } while (0)
#define PARTSM(P0, P1, s_, alpha) do { \
        const int kbase_ = 128 * (s_) + 64 * par; \
        if (kbase_ + 63 > qrow0) { const int dq = qrow0 + r32 - kbase_ - 4 * hi; const float NEG_ = -__builtin_inff(); \
            _Pragma("unroll") for (int r = 0; r < 16; ++r) { const int c = (r & 3) + 8 * (r >> 2); \
                if (dq - c < 0) P0[r] = NEG_; if (dq - c - 32 < 0) P1[r] = NEG_; } } \
        float pmax = P0[0]; \
        _Pragma("unroll") for (int r = 1; r < 16; ++r) pmax = fmaxf(pmax, P0[r]); \
        _Pragma("unroll") for (int r = 0; r < 16; ++r) pmax = fmaxf(pmax, P1[r]); \
        { auto rr = __builtin_amdgcn_permlane32_swap(__float_as_uint(pmax), __float_as_uint(pmax), false, false); \
          pmax = fmaxf(__uint_as_float(rr[0]), __uint_as_float(rr[1])); } \
          \
        if (__builtin_expect(__all((pmax - m_reg) * SCALE <= 8.f), 1)) { alpha = 1.f; } \
        else { const float mn = fmaxf(m_reg, pmax); alpha = __builtin_amdgcn_exp2f((m_reg - mn) * C2); m_reg = mn; } \
        const float mnL = -m_reg * C2; \
        _Pragma("unroll") for (int r = 0; r < 16; ++r) { P0[r] = __builtin_amdgcn_exp2f(fmaf(P0[r], C2, mnL)); P1[r] = fmaf(P1[r], C2, mnL); } } while (0)
#define PK4(P, B_, OUT) do { unsigned a0 = cvtpk(P[B_+0], P[B_+1]), a1 = cvtpk(P[B_+2], P[B_+3]);                          \
        unsigned b0 = cvtpk(P[B_+4], P[B_+5]), b1 = cvtpk(P[B_+6], P[B_+7]);                                             \
        auto r0 = __builtin_amdgcn_permlane32_swap(a0, b0, false, false); auto r1 = __builtin_amdgcn_permlane32_swap(a1, b1, false, false); \
        u32x4 w = {r0[0], r1[0], r0[1], r1[1]}; OUT = *reinterpret_cast<bf16x8*>(&w); } while (0)
#define FINSM(P0, P1, alpha) do { float ps = 0.f; \
        _Pragma("unroll") for (int r = 0; r < 16; ++r) { P1[r] = __builtin_amdgcn_exp2f(P1[r]); ps += P0[r]; } \
        _Pragma("unroll") for (int r = 0; r < 16; ++r) ps += P1[r]; \
        { auto rr = __builtin_amdgcn_permlane32_swap(__float_as_uint(ps), __float_as_uint(ps), false, false); \
          ps = __uint_as_float(rr[0]) + __uint_as_float(rr[1]); } \
        l_reg = l_reg * (alpha) + ps; \
        PK4(P0, 0, pa0); PK4(P0, 8, pa1); PK4(P1, 0, pa2); PK4(P1, 8, pa3); } while (0)
#define RESC(a) do { if (__any((a) < 1.f)) { if (hi == 0) al_l[r32] = (a); asm volatile("s_waitcnt lgkmcnt(0)" ::: "memory"); \
        _Pragma("unroll") for (int d_ = 0; d_ < 4; ++d_) _Pragma("unroll") for (int r = 0; r < 16; ++r) o[d_][r] *= al_l[crow(r, hi)]; } } while (0)
#define TRRD(dst, off) asm volatile("ds_read_b64_tr_b16 %0, %1 offset:%2" : "=&v"(dst) : "v"(vb), "i"(off) : "memory")
#define PV_D0(d0) do { s16x4 l0, l1, l2, l3, h0, h1, h2, h3; constexpr int b_ = (d0) * 512; \
        TRRD(l0, b_); TRRD(h0, b_ + 2048); TRRD(l1, b_ + 4096); TRRD(h1, b_ + 6144); TRRD(l2, b_ + 8192); TRRD(h2, b_ + 10240); TRRD(l3, b_ + 12288); TRRD(h3, b_ + 14336); \
        asm volatile("s_waitcnt lgkmcnt(0)" ::: "memory"); SBAR(); \
        o[d0] = __builtin_amdgcn_mfma_f32_32x32x16_bf16(pa0, (bf16x8){l0[0], l0[1], l0[2], l0[3], h0[0], h0[1], h0[2], h0[3]}, o[d0], 0, 0, 0);   \
        o[d0] = __builtin_amdgcn_mfma_f32_32x32x16_bf16(pa1, (bf16x8){l1[0], l1[1], l1[2], l1[3], h1[0], h1[1], h1[2], h1[3]}, o[d0], 0, 0, 0);   \
        o[d0] = __builtin_amdgcn_mfma_f32_32x32x16_bf16(pa2, (bf16x8){l2[0], l2[1], l2[2], l2[3], h2[0], h2[1], h2[2], h2[3]}, o[d0], 0, 0, 0);   \
        o[d0] = __builtin_amdgcn_mfma_f32_32x32x16_bf16(pa3, (bf16x8){l3[0], l3[1], l3[2], l3[3], h3[0], h3[1], h3[2], h3[3]}, o[d0], 0, 0, 0); } while (0)
#define PV(buf) do { const int vb = vb0 + (buf) * 32768; PV_D0(0); PV_D0(1); PV_D0(2); PV_D0(3); } while (0)
#define ACT(s_) (128 * (s_) + 64 * par <= qrow0 + 31)
#define ITER(PX0, PX1, alX, PY0, PY1, alY, s_, E) do { \
        asm volatile("s_waitcnt vmcnt(0)" ::: "memory"); __syncthreads(); \
        if ((s_) + 2 < nsteps) stageK((s_) + 2, E); \
        stageV((s_) + 1, 1 - (E)); SBAR(); \
        QKT(PX0, PX1, 1 - (E), ACT((s_) + 1)); \
        FINSM(PY0, PY1, alY); SBAR(); \
        PV(E); PARTSM(PX0, PX1, (s_) + 1, alX); \
        RESC(alX); } while (0)

    for (int pass = 0; pass < 2; ++pass) {
        const int qb = pass ? 15 - xq : xq, nsteps = qb + 1;
        const int qrow0 = qb * 128 + rg * 32;
        int tz = tid; asm volatile("" : "+v"(tz));
        int r32 = tz & 31, hi = (tz >> 5) & 1;
        const int vb0 = (int)(uintptr_t)V_lds + par * 16384 + v_rd_base(tz & 63);
        koff = (unsigned)((tz >> 4) * 256 + (((tz & 15) ^ ((tz >> 4) & 7)) << 4)); loff = (unsigned)tz * 16;
        { const int sub = tz >> 5, r7 = (tz >> 2) & 7, c8 = tz & 3, kk = (sub >> 2) * 8 + r7;
          const int k = (kk & ~0xC) | ((kk & 4) << 1) | ((kk & 8) >> 1);
          voff = (unsigned)(k * 256 + ((sub & 3) * 32 + c8 * 8) * 2); }
        bf16x8 qr[8];
#pragma unroll
        for (int d0 = 0; d0 < 8; ++d0) qr[d0] = *(const bf16x8*)(Q + ((size_t)h * SEQ + qrow0 + r32) * HD + d0 * 16 + hi * 8);
        float m_reg = -1e30f, l_reg = 0.f; f32x16 o[4] = {};
        f32x16 pA0, pA1, pB0, pB1; float alA, alB; bf16x8 pa0, pa1, pa2, pa3;
        __syncthreads();
        stageK(0, 0); stageV(0, 0);
        asm volatile("s_waitcnt vmcnt(0)" ::: "memory"); __syncthreads();
        if (nsteps > 1) stageK(1, 1);
        QKT(pA0, pA1, 0, ACT(0)); PARTSM(pA0, pA1, 0, alA);
        int s = 0;
        for (; s + 2 < nsteps; s += 2) {
            ITER(pB0, pB1, alB, pA0, pA1, alA, s, 0);
            ITER(pA0, pA1, alA, pB0, pB1, alB, s + 1, 1);
        }
        if (s + 1 < nsteps) {
            ITER(pB0, pB1, alB, pA0, pA1, alA, s, 0);
            asm volatile("s_waitcnt vmcnt(0)" ::: "memory"); __syncthreads();
            FINSM(pB0, pB1, alB); SBAR(); PV(1);
        } else {
            asm volatile("s_waitcnt vmcnt(0)" ::: "memory"); __syncthreads();
            FINSM(pA0, pA1, alA); SBAR(); PV(0);
        }
        __syncthreads();
        tz = tid; asm volatile("" : "+v"(tz)); r32 = tz & 31; hi = (tz >> 5) & 1;
        float* cO = (float*)lds + rg * (32 * 128 + 64);
        if (par == 1) {
#pragma unroll
            for (int d = 0; d < 4; ++d)
#pragma unroll
                for (int r = 0; r < 16; ++r) cO[crow(r, hi) * 128 + d * 32 + r32] = o[d][r];
            if (hi == 0) { cO[4096 + r32] = m_reg; cO[4096 + 32 + r32] = l_reg; }
        }
        __syncthreads();
        if (par == 0) {
            const float m1 = cO[4096 + r32], l1 = cO[4096 + 32 + r32];
            const float mm = fmaxf(m_reg, m1);
            const float a0 = __builtin_amdgcn_exp2f((m_reg - mm) * C2), a1 = __builtin_amdgcn_exp2f((m1 - mm) * C2);
            const float linv = 1.f / (l_reg * a0 + l1 * a1);
            if (hi == 0) { al_l[r32] = a0 * linv; al_l[32 + r32] = a1 * linv; }
            asm volatile("s_waitcnt lgkmcnt(0)" ::: "memory");
#pragma unroll
            for (int r = 0; r < 16; ++r) {
                const int orow = crow(r, hi); const float c0 = al_l[orow], c1 = al_l[32 + orow];
#pragma unroll
                for (int d = 0; d < 4; ++d) {
                    const float v = o[d][r] * c0 + cO[orow * 128 + d * 32 + r32] * c1;
                    const float vn = __shfl_xor(v, 1);
                    if ((r32 & 1) == 0) *(unsigned*)(ctx + (size_t)(qrow0 + orow) * (NH * HD) + h * HD + d * 32 + r32) = cvtpk(v, vn);
                }
            }
        }
    }
}

extern "C" void kernel_launch(void* const* d_in, const int* in_sizes, int n_in, void* d_out, int out_size, void* d_ws, size_t ws_size,
                              hipStream_t stream) {
    const u16* x = (const u16*)d_in[0]; const u16* ckv = (const u16*)d_in[1];
    const u16* wq = (const u16*)d_in[2]; const u16* wuk = (const u16*)d_in[3]; const u16* wuv = (const u16*)d_in[4]; const u16* wo = (const u16*)d_in[5];
    const u16* w1 = (const u16*)d_in[7]; const u16* b1 = (const u16*)d_in[8]; const u16* w2 = (const u16*)d_in[9]; const u16* b2 = (const u16*)d_in[10];
    u16* out = (u16*)d_out;
    char* ws = (char*)d_ws;
    u16* xn = (u16*)ws;                                   ws += (size_t)SEQ * DM * 2;
    u16* qb = (u16*)ws;                                   ws += (size_t)SEQ * NH * HD * 2;
    u16* kb = (u16*)ws;                                   ws += (size_t)SEQ * NH * HD * 2;
    u16* vb = (u16*)ws;                                   ws += (size_t)SEQ * NH * HD * 2;
    u16* ctx = (u16*)ws;                                  ws += (size_t)SEQ * NH * HD * 2;
    u16* hbuf = (u16*)ws;                                 ws += (size_t)SEQ * DM * 2;
    u16* hn = (u16*)ws;                                   ws += (size_t)SEQ * DM * 2;
    u16* fbuf = (u16*)ws;                                 ws += (size_t)SEQ * DFFP * 2;
    float* part = (float*)ws;                             ws += (size_t)4 * SEQ * DM * 4;

    constexpr int KS = 4;
    auto gq = gemm_kernel<128, 256, 2, 4, EPI_QKV, 3>;
    auto go = gemm_kernel<64, 128, 2, 2, EPI_PART, 3>;
    auto g1 = gemm_kernel<64, 128, 2, 2, EPI_GELU, 3>;
    auto g2 = gemm_kernel<64, 64, 2, 2, EPI_FFN2, 3>;
    (void)hipFuncSetAttribute((const void*)gq, hipFuncAttributeMaxDynamicSharedMemorySize, 3 * (128 + 256) * 128 + 8 * 16 * 128);
    (void)hipFuncSetAttribute((const void*)go, hipFuncAttributeMaxDynamicSharedMemorySize, 3 * (64 + 128) * 128);
    (void)hipFuncSetAttribute((const void*)g1, hipFuncAttributeMaxDynamicSharedMemorySize, 3 * (64 + 128) * 128);
    (void)hipFuncSetAttribute((const void*)g2, hipFuncAttributeMaxDynamicSharedMemorySize, 3 * (64 + 64) * 128);
    (void)hipFuncSetAttribute((const void*)attn_kernel, hipFuncAttributeMaxDynamicSharedMemorySize, ATT_LDS);

    ln1_kernel<<<SEQ / 4, 256, 0, stream>>>(x, xn);
    {
        GemmArgs g = {};
        g.A[0] = xn; g.A[1] = ckv; g.A[2] = ckv; g.B[0] = wq; g.B[1] = wuk; g.B[2] = wuv; g.C[0] = qb; g.C[1] = kb; g.C[2] = vb;
        g.lda = DM; g.ldb = NH * HD; g.M = SEQ; g.N = NH * HD; g.K = DM; g.Kv = DM; g.ksplit = 1;
        g.nTn = (NH * HD) / 256;
        g.ksplit = 3; gq<<<256, 512, 3 * (128 + 256) * 128 + 8 * 16 * 128, stream>>>(g);
    }
    attn_kernel<<<NH * 8, 512, ATT_LDS, stream>>>(qb, kb, vb, ctx);
    {
        GemmArgs g = {};
        g.A[0] = ctx; g.B[0] = wo; g.C[0] = part; g.lda = NH * HD; g.ldb = DM; g.M = SEQ; g.N = DM; g.K = NH * HD / KS; g.Kv = NH * HD;
        g.nTn = DM / 128; g.ksplit = KS;
        go<<<512, 256, 3 * (64 + 128) * 128, stream>>>(g);
    }
    oreduce_ln2_kernel<KS><<<SEQ / 4, 256, 0, stream>>>(part, x, hbuf, hn);
    {
        GemmArgs g = {};
        g.A[0] = hn; g.B[0] = w1; g.C[0] = fbuf; g.bias = b1; g.lda = DM; g.ldb = DFF; g.M = SEQ; g.N = DFF; g.K = DM; g.Kv = DM;
        g.nTn = (DFF + 127) / 128; g.ksplit = 1;
        g1<<<352, 256, 3 * (64 + 128) * 128, stream>>>(g);
    }
    {
        GemmArgs g = {};
        g.A[0] = fbuf; g.B[0] = w2; g.C[0] = out; g.bias = b2; g.res = hbuf; g.lda = DFFP; g.ldb = DM; g.M = SEQ; g.N = DM; g.K = DFFP; g.Kv = DFF;
        g.nTn = DM / 64; g.ksplit = 1;
        g2<<<256, 256, 3 * (64 + 64) * 128, stream>>>(g);
    }
}
```
